```python
import torch

def _softmax(x, axis=-1):
    return torch.softmax(x, dim=axis)

def _gelu(x):
    return torch.nn.functional.gelu(x, approximate='tanh')

def _silu(x):
    return torch.nn.functional.silu(x)

def _sigmoid(x):
    return torch.sigmoid(x)

def _layernorm(x, axis=-1, eps=1e-05):
    m = x.mean(dim=axis, keepdim=True)
    v = x.var(dim=axis, keepdim=True, unbiased=False)
    return (x - m) / torch.sqrt(v + eps)

def _scatter_add(tgt, idx, src, axis=0):
    return tgt.clone().index_add_(axis, idx, src)

def _segment_sum(src, seg, n):
    out = torch.zeros((n,) + src.shape[1:], dtype=src.dtype)
    return out.index_add_(0, seg, src)

def _pad(x, pw):
    flat = tuple((v for pair in reversed(pw) for v in pair))
    return torch.nn.functional.pad(x, flat)
kernel_name = 'sequential_preLN__memory_aug_recurrent_mem_tr__mixer_gmlp__prompt'

def setup_inputs():

    _w = lambda a: (lambda t: t.to(torch.bfloat16) if t.is_floating_point() else t)(torch.as_tensor(a))

    def _A_setup():
        B, S, Dm = (1, 2048, 512)
        H, Dh = (32, 128)
        M, seg = (8, 32)
        seg = min(seg, S)
        S = S // seg * seg
        return {'x': _w((lambda *s: torch.randn(*s, dtype=torch.bfloat16) * 0.05)(B, S, Dm)), 'mem0': _w((lambda *s: torch.randn(*s, dtype=torch.bfloat16) * 0.2)(B, M, Dm)), 'Wq': _w((lambda *s: torch.randn(*s, dtype=torch.bfloat16) * 1.6)(Dm, H, Dh) * Dm ** (-0.5)), 'Wk': _w((lambda *s: torch.randn(*s, dtype=torch.bfloat16) * 1.6)(Dm, H, Dh) * Dm ** (-0.5)), 'Wv': _w((lambda *s: torch.randn(*s, dtype=torch.bfloat16) * 0.2)(Dm, H, Dh) * Dm ** (-0.5)), 'Wo': _w((lambda *s: torch.randn(*s, dtype=torch.bfloat16) * 0.2)(H, Dh, Dm) * (H * Dh) ** (-0.5)), 'seg_len': seg}
    _A_in = _A_setup()

    def _B_setup():
        B, S, D, F = (1, 2048, 512, 1376)
        Fh = F // 2
        return {'x': _w((lambda *s: torch.randn(*s, dtype=torch.bfloat16) * 0.05)(B, S, D)), 'w_in': _w((lambda *s: torch.randn(*s, dtype=torch.bfloat16) * 0.4)(D, 2 * Fh)), 'w_sp': _w(0.01 * (lambda *s: torch.randn(*s, dtype=torch.bfloat16) * 0.4)(S, S)), 'b_sp': _w(1.0 + 0.01 * (lambda *s: torch.randn(*s, dtype=torch.bfloat16) * 0.05)(S)), 'w_out': _w((lambda *s: torch.randn(*s, dtype=torch.bfloat16) * 0.05)(Fh, D))}
    _B_in = _B_setup()
    return {'A_x': _A_in['x'], 'A_mem0': _A_in['mem0'], 'A_Wq': _A_in['Wq'], 'A_Wk': _A_in['Wk'], 'A_Wv': _A_in['Wv'], 'A_Wo': _A_in['Wo'], 'A_seg_len': _A_in['seg_len'], 'B_x': _B_in['x'], 'B_w_in': _B_in['w_in'], 'B_w_sp': _B_in['w_sp'], 'B_b_sp': _B_in['b_sp'], 'B_w_out': _B_in['w_out']}

def reference(A_x, A_mem0, A_Wq, A_Wk, A_Wv, A_Wo, A_seg_len, B_x, B_w_in, B_w_sp, B_b_sp, B_w_out):

    def _A_ref(x, mem0, Wq, Wk, Wv, Wo, seg_len):
        B, S, Dm = x.shape
        H, Dh = (Wq.shape[1], Wq.shape[2])
        M = mem0.shape[1]
        scale = Dh ** (-0.5)
        n_seg = S // seg_len
        L = M + seg_len
        idx = torch.arange(L)
        mask = (idx[:, None] < M) | (idx[:, None] >= idx[None, :])
        mem = mem0
        outs = []
        for s in range(n_seg):
            xs = x[:, s * seg_len:(s + 1) * seg_len, :].clone()
            h = torch.concatenate([mem, xs], axis=1)
            q = torch.einsum('btd,dhe->bhte', h, Wq)
            k = torch.einsum('btd,dhe->bhte', h, Wk)
            v = torch.einsum('btd,dhe->bhte', h, Wv)
            scores = torch.einsum('bhqe,bhke->bhqk', q, k) * scale
            scores = torch.where(mask[None, None], scores, -1000000000.0)
            p = _softmax(scores, axis=-1)
            o = torch.einsum('bhqk,bhke->bhqe', p, v)
            o = torch.einsum('bhte,hed->btd', o, Wo)
            mem = o[:, :M, :].clone()
            outs.append(o[:, M:, :])
        return torch.concatenate(outs, axis=1)

    def _B_ref(x, w_in, w_sp, b_sp, w_out):
        B, S, D = x.shape
        F = w_in.shape[-1]
        Fh = F // 2
        z = _gelu(torch.einsum('bsd,df->bsf', x, w_in))
        z1 = z[..., :Fh].clone()
        z2 = _layernorm(z[..., Fh:], axis=-1)
        g = torch.einsum('ts,bsf->btf', w_sp, z2) + b_sp[None, :, None]
        return torch.einsum('bsf,fd->bsd', z1 * g, w_out)
    x = A_x
    h = x + _A_ref(_layernorm(x, axis=-1), A_mem0, A_Wq, A_Wk, A_Wv, A_Wo, A_seg_len)
    h2 = h + _B_ref(_layernorm(h, axis=-1), B_w_in, B_w_sp, B_b_sp, B_w_out)
    return h2
```

```cpp
#include <hip/hip_runtime.h>
#include <hip/hip_bf16.h>
typedef unsigned u32;
typedef unsigned long long u64;
typedef __attribute__((ext_vector_type(2))) u32 u32x2;
typedef __attribute__((ext_vector_type(4))) u32 u32x4;
typedef __attribute__((ext_vector_type(8))) u32 u32x8;
typedef __attribute__((ext_vector_type(2))) float f32x2;
typedef __attribute__((ext_vector_type(4))) float f32x4;
typedef __attribute__((ext_vector_type(2))) __bf16 bfx2;
typedef __attribute__((ext_vector_type(8))) __bf16 bfx8;
typedef __amdgpu_buffer_rsrc_t rsrc_t;
#define DI __device__ __forceinline__
#define UNR _Pragma("unroll")
#define SB() __builtin_amdgcn_sched_barrier(0)
#define SENT 0xFFFFFFFFu
#define SPINCAP 400000
#define MB (1u<<20)
#define O_M   (3*MB)
#define O_FL  (4*MB)
#define F_SEL 0
#define F_KV  1024
#define F_KM  (F_KV+8192)
#define F_O   (F_KM+8192)
#define F_H   (F_O+8192)
#define F_Z   (F_H+16384)
#define F_U   (F_Z+8192)
#define FL_SZ 65536
#define O_XF  (5*MB)
#define O_HF  (7*MB)
#define O_Z1  (9*MB)
#define O_Z2  (12*MB)
#define O_UF  (15*MB)
#define O_ST  (18*MB)
#define O_KM  (20*MB)
#define O_KV  (24*MB)
#define O_VM  (56*MB)
#define O_OF  (72*MB)
#define WS_SZ (88*MB)
#define LDS_SZ (148*1024)

DI rsrc_t mk(const void* p,u32 n){ return __builtin_amdgcn_make_buffer_rsrc((void*)p,0,n,0x00020000); }
DI u32x4 ld4(rsrc_t r,u32 v,u32 s){ return __builtin_amdgcn_raw_buffer_load_b128(r,v,s,0); }
DI u32x2 ld2(rsrc_t r,u32 v,u32 s){ return __builtin_amdgcn_raw_buffer_load_b64(r,v,s,0); }
DI u32   ld1(rsrc_t r,u32 v,u32 s){ return __builtin_amdgcn_raw_buffer_load_b32(r,v,s,0); }
DI u32x4 ld4c(rsrc_t r,u32 v,u32 s){ return __builtin_amdgcn_raw_buffer_load_b128(r,v,s,16); }
DI u32x2 ld2c(rsrc_t r,u32 v,u32 s){ return __builtin_amdgcn_raw_buffer_load_b64(r,v,s,16); }
DI u32   ld1c(rsrc_t r,u32 v,u32 s){ return __builtin_amdgcn_raw_buffer_load_b32(r,v,s,16); }
DI void st4c(rsrc_t r,u32 v,u32 s,u32x4 d){ __builtin_amdgcn_raw_buffer_store_b128(d,r,v,s,16); }
DI void st2c(rsrc_t r,u32 v,u32 s,u32x2 d){ __builtin_amdgcn_raw_buffer_store_b64(d,r,v,s,16); }
DI void st1c(rsrc_t r,u32 v,u32 s,u32 d){ __builtin_amdgcn_raw_buffer_store_b32(d,r,v,s,16); }
DI void dma16(rsrc_t r,u32 v,u32 s,u32 lds){ asm volatile("s_mov_b32 m0, %3\n s_nop 0\n buffer_load_dwordx4 %0, %1, %2 offen sc1 lds"::"v"(v),"s"(r),"s"(s),"s"(lds):"memory"); }
DI void vm0(){ asm volatile("s_waitcnt vmcnt(0)":::"memory"); }
DI void bar(){ asm volatile("s_waitcnt lgkmcnt(0)\n s_barrier":::"memory"); }
DI u32 pk(float a,float b){ f32x2 v={a,b}; return __builtin_bit_cast(u32,__builtin_convertvector(v,bfx2)); }
DI float blo(u32 v){ return __uint_as_float(v<<16); }
DI float bhi(u32 v){ return __uint_as_float(v&0xFFFF0000u); }
DI u32 plo(u32 a,u32 b){ return __builtin_amdgcn_perm(b,a,0x05040100u); }
DI u32 phi(u32 a,u32 b){ return __builtin_amdgcn_perm(b,a,0x07060302u); }
DI f32x4 mfma(u32x4 a,u32x4 b,f32x4 c){ return __builtin_amdgcn_mfma_f32_16x16x32_bf16(__builtin_bit_cast(bfx8,a),__builtin_bit_cast(bfx8,b),c,0,0,0); }
DI float gmax(float x){ u32 u=__float_as_uint(x); auto a=__builtin_amdgcn_permlane16_swap(u,u,false,false); x=fmaxf(__uint_as_float(a[0]),__uint_as_float(a[1]));
  u=__float_as_uint(x); auto b=__builtin_amdgcn_permlane32_swap(u,u,false,false); return fmaxf(__uint_as_float(b[0]),__uint_as_float(b[1])); }
DI float gsum(float x){ u32 u=__float_as_uint(x); auto a=__builtin_amdgcn_permlane16_swap(u,u,false,false); x=__uint_as_float(a[0])+__uint_as_float(a[1]);
  u=__float_as_uint(x); auto b=__builtin_amdgcn_permlane32_swap(u,u,false,false); return __uint_as_float(b[0])+__uint_as_float(b[1]); }
template<int C> DI float dppf(float x){ return __int_as_float(__builtin_amdgcn_update_dpp(0,__float_as_int(x),C,0xF,0xF,true)); }
DI float rsum16(float x){ x+=dppf<0x128>(x); x+=dppf<0x124>(x); x+=dppf<0x122>(x); x+=dppf<0x121>(x); return x; }
template<class T> DI T toA(T v){ T o; asm("":"=a"(o):"0"(v)); return o; }
#define LDS(T,off) (*(__attribute__((address_space(3))) T*)(size_t)(off))
DI int lane(){ int l; asm volatile("v_mbcnt_lo_u32_b32 %0, -1, 0\n v_mbcnt_hi_u32_b32 %0, -1, %0":"=v"(l)); __builtin_assume(l>=0&&l<64); return l; }
DI void waitf(rsrc_t r,u32 off,u32 n,u32 need,int& dead){
  u32 vo=n>1?(lane()&(n-1))*4:0; int sp=0;
  while(!dead){ u32 v=ld1c(r,vo,off); if(!__any(v<need)) break; if(++sp>SPINCAP){dead=1;break;} __builtin_amdgcn_s_sleep(2); }
  asm volatile("":::"memory");
}
DI void mkfr(const u32* t,u32x4& f0,u32x4& f1){
  f0=u32x4{plo(t[0],t[1]),plo(t[2],t[3]),plo(t[4],t[5]),plo(t[6],t[7])};
  f1=u32x4{phi(t[0],t[1]),phi(t[2],t[3]),phi(t[4],t[5]),phi(t[6],t[7])};
}
template<int NKS,int NA> DI void ldw(u32x4 (*w)[2],rsrc_t r,u32 vo,u32 so,u32 rs){
  u32 t[2][24]; asm volatile("":"+s"(so));
  constexpr int NB=(NKS+2)/3;
  UNR for(int b=0;b<=NB;b++){
    if(b<NB){ UNR for(int i=0;i<24;i++) if(b*3+i/8<NKS) t[b&1][i]=ld1(r,vo,so+((b*3+i/8)*32+(i&7))*rs); }
    SB();
    if(b>0){ UNR for(int q=0;q<3;q++){ int ks=(b-1)*3+q; if(ks<NKS){ u32x4 f0,f1; mkfr(&t[(b-1)&1][q*8],f0,f1);
      if(ks<NA){ w[ks][0]=toA(f0); w[ks][1]=toA(f1);} else { w[ks][0]=f0; w[ks][1]=f1; } } } }
    SB();
  }
}
#define TC(i,v)
#define CNT(c,v)
#define TS(i)
struct Args{ const void *x,*mem0,*wq,*wk,*wv,*wo,*win,*wsp,*bsp,*wout; void *out,*ws; };
DI char* wsbase(void* ws){ return (char*)(((size_t)ws+(2*MB-1))&~(size_t)(2*MB-1)); }

__global__ void __launch_bounds__(256) pre_kernel(Args A){
  char* ws=wsbase(A.ws); rsrc_t rw=mk(ws,WS_SZ);
  int b=blockIdx.x,t=threadIdx.x,l=t&63,w=t>>6;
  u32x4 xv[2];
  UNR for(int i=0;i<2;i++) xv[i]=((const u32x4*)A.x)[(b*8+w*2+i)*64+l];
  if((b&127)==0){
    if(w==0){
      if(l<8) st1c(rw,l*2048,b*16384,SENT);
      vm0(); u32 mn[2]={~0u,~0u};
      UNR for(int i=0;i<8;i++){ int j=(i>>1)+(i&1)*4;
        u64 c0=__builtin_amdgcn_s_memtime(); asm volatile("s_waitcnt lgkmcnt(0)":::"memory");
        u32 v=ld1c(rw,0,b*16384+j*2048); asm volatile("s_waitcnt vmcnt(0)":"+v"(v)::"memory");
        u64 c1=__builtin_amdgcn_s_memtime(); mn[i&1]=min(mn[i&1],(u32)(c1-c0)); }
      if(l==0) *(u32*)(ws+O_FL+F_SEL+(b>>7)*4)=mn[1]<mn[0]?8192u:0u;
    }
    __syncthreads();
  }
  UNR for(int i=0;i<4;i++){ u32x4 v={SENT,SENT,SENT,SENT}; int c=i*256+t;
    if(b==192){ int cc=c&511; const u32x2* p=(const u32x2*)A.mem0+((cc>>2)&7)*128+(cc>>6)*16+(cc&3)*4+((cc>>5)&1); u32x2 a=p[0],e=p[2]; v=u32x4{a.x,a.y,e.x,e.y}; }
    ((u32x4*)ws)[b*1024+c]=v; }
  if(b>=4&&t<16) ((u32x4*)(ws+O_FL))[b*16+t]=u32x4{0,0,0,0};
  UNR for(int i=0;i<2;i++){
    float f[8],s=0; UNR for(int j=0;j<4;j++){ f[2*j]=blo(xv[i][j]); f[2*j+1]=bhi(xv[i][j]); s+=f[2*j]+f[2*j+1]; }
    float mu=gsum(rsum16(s))*(1.f/512),q=0; UNR for(int j=0;j<8;j++){ f[j]-=mu; q+=f[j]*f[j]; }
    float rs=rsqrtf(gsum(rsum16(q))*(1.f/512)+1e-5f);
    int tk=b*8+w*2+i,tau=tk&31,kt=(tau>>2)&1,c=4*(tau>>3)+(tau&3);
    ((u32x4*)(ws+O_XF))[(((tk>>5)*2+kt)*16+(l>>2))*64+(l&3)*16+c]=u32x4{pk(f[0]*rs,f[1]*rs),pk(f[2]*rs,f[3]*rs),pk(f[4]*rs,f[5]*rs),pk(f[6]*rs,f[7]*rs)};
  }
}
#define PIN(x) asm volatile("":"+v"(x))
#define DL1 6
#define DL2 7
#define DL3 7
#define DL4 0
#define OPT 4
#define STR_(x) #x
#define STR(x) STR_(x)
#define CEXP 0.12751743f
typedef __attribute__((ext_vector_type(16))) u32 u32x16;
typedef __attribute__((ext_vector_type(32))) u32 u32x32;
#define MT(i)
#define MTACC
#define CLOB "v128","v129","v130","v131","v132","v133","v134","v135","v136","v137","v138","v139","v140","v141","v142","v143","v144","v145","v146","v147","v148","v149","v150","v151","v152","v153","v154","v155","v156","v157","v158","v159","v160","v161","v162","v163","v164","v165","v166","v167","v168","v169","v170","v171","v172","v173","v174","v175","v176","v177","v178","v179","v180","v181","v182","v183","v184","v185","v186","v187","v188","v189","v190","v191","v192","v193","v194","v195","v196","v197","v198","v199","v200","v201","v202","v203","v204","v205","v206","v207","v208","v209","v210","v211","v212","v213","v214","v215","v216","v217","v218","v219","v220","v221","v222","v223","v224","v225","v226","v227","v228","v229","v230","v231","v232","v233","v234","v235","v236","v237","v238","v239","v240","v241","v242","v243","v244","v245","v246","v247","s40","s41","s42","s43","s44","s45","s46","s47","s48","s49","s50","s51","s52","s53","s54","s55","s56","s57","s58","s59","s60","s61","s62","s63","s80","s81","s82","s83","s84","s85","s86","s87"
DI void roleR(const Args& A,char* ws,int h,int th,const int w,int& dead){
  const int l=lane(),g=l>>4,n=l&15;
  rsrc_t rw=mk(ws,WS_SZ);
  u32x32 wa[8],wv[2],wo[2];
  { rsrc_t rq=mk(A.wq,4*MB),rk=mk(A.wk,4*MB),rv=mk(A.wv,4*MB),ro=mk(A.wo,4*MB);
    const u32 rl=(32*(g>>1)+8*(g&1))*8192+h*256; u32 t[2][32];
#define ROWJ(j) ((16*(((j)>>1)&1)+4*(((j)>>3)&1)+2*(((j)>>2)&1)+((j)&1))*8192)
    UNR for(int b=0;b<=8;b++){
      if(b<8){ u32 so=((2*b+2*w)&7)*64*8192+w*64; asm volatile("":"+s"(so));
        UNR for(int j=0;j<32;j++) t[b&1][j]=ld1(b<4?rq:rk,rl+n*4,so+(j>>4)*64*8192+ROWJ(j)); }
      SB();
      if(b>0){ u32x32 f; const u32* p=t[(b-1)&1];
        UNR for(int j=0;j<32;j++){ int o=(j>>4)*16+(j&7)*2; f[j]=(j&8)?phi(p[o],p[o+1]):plo(p[o],p[o+1]); }
#define PA(i,r) if(b==i+1) asm volatile("":"={a[" r "]}"(wa[i]):"0"(f));
        PA(0,"0:31") PA(1,"32:63") PA(2,"64:95") PA(3,"96:127") PA(4,"128:159") PA(5,"160:191") PA(6,"192:223") PA(7,"224:255") }
      SB();
    }
    u32x2 v[32]; u32x4 u[16];
    UNR for(int j=0;j<32;j++) v[j]=ld2(rv,rl+n*8,(2*w+(j>>4))*64*8192+th*128+ROWJ(j));
    UNR for(int j=0;j<16;j++) u[j]=ld4(ro,g*8192+n*16,(h*128+th*64+(j&7)+32*(j>>3))*1024+w*256);
    UNR for(int q=0;q<8;q++) UNR for(int j=0;j<8;j++){ const u32x2* p=v+(q>>2)*16+2*j; int hi=(q>>1)&1;
      wv[q>>2][(q&3)*8+j]=(q&1)?phi(p[0][hi],p[1][hi]):plo(p[0][hi],p[1][hi]); }
    asm volatile("":"+{v[0:31]}"(wv[0]),"+{v[32:63]}"(wv[1])); SB();
    UNR for(int q=0;q<8;q++) UNR for(int j=0;j<8;j++) wo[q>>2][(q&3)*8+j]=(q&1)?phi(u[2*j][q>>1],u[2*j+1][q>>1]):plo(u[2*j][q>>1],u[2*j+1][q>>1]);
    asm volatile("":"+{v[64:95]}"(wo[0]),"+{v[96:127]}"(wo[1]));
  }
  const u32 sel0=__builtin_amdgcn_readfirstlane(ld1(rw,0,O_FL+F_SEL)),sel1=__builtin_amdgcn_readfirstlane(ld1(rw,0,O_FL+F_SEL+4));
  const int pi=2*h+th,p=(n>>1)&1; const u32 l16=l*16;
  { u32x4 sel; UNR for(int j=0;j<4;j++) sel[j]=(n&3)==j?((n&4)?0x3F800000u:0x3F80u):0u; LDS(u32x4,17408+l16)=sel; LDS(u32x4,18432)=u32x4{0x3F803F80u,0x3F803F80u,0x3F80u,0}; }
  const int rho=(2*w+(n>>3))*8+g;
  u32x8 vc={p?0xEEEEu:0x4444u, l16, rho*16384+((pi+rho+(rho&3))&63)*128+(n&7)*16u, 8192+2*g*288+w*64+n*4u,
            8192+(2*(n>>2)+(n&1))*288+g*16u, 32768+l16, w*2048+((g>>1)*4+(n>>2))*128+(n&1)*64+(2*(g&1)+p)*16u,
            w==0?g*64+n*8u:w==3?10496+(4*th+g)*288+n*16u:13312+(w-1)*2048+l16};
  const u32 kvs=O_KV+h*16384+w*3072;
  TS(1) waitf(rw,O_FL+F_KV+2*h*128,1,1,dead); TS(2)
  UNR for(int i=0;i<3;i++) dma16(rw,l16,kvs+i*1024+(3*w+i>=8?th*4096:0),32768+(3*w+i)*1024);
  u32x16 sc={(u32)w, sel0, MB+sel0, 2*MB+sel1, O_M+sel1, pi*16384u, O_KV+h*16384+MB/2+(w-1+(w==3?th:0))*4096, pi*16384u+(w==3?5120:(w-1)*3072),
             O_FL+F_KV+(2*h+1)*128u, O_FL+F_KM+pi*128u, w==3?O_KM+h*2048+th*1024:O_VM+pi*4096+(w-1)*2048u, w==3?65536u:262144u,
             49152+(w-1)*4096u, (u32)dead, 0, 0};
  u32x8 pc={};
  asm volatile(
".macro SQ c,a,w\n"
" v_smfmac_f32_16x16x64_bf16 v[\\c:\\c+3],v[\\a:\\a+3],a[\\w:\\w+7],v248\n"
".endm\n"
".macro SV c,a,w\n"
" v_smfmac_f32_16x16x64_bf16 v[\\c:\\c+3],v[\\a:\\a+3],v[\\w:\\w+7],v248\n"
".endm\n"
".macro MZ d,a,b\n"
" v_mfma_f32_16x16x32_bf16 v[\\d:\\d+3],v[\\a:\\a+3],v[\\b:\\b+3],0\n"
".endm\n"
".macro MA d,a,b\n"
" v_mfma_f32_16x16x32_bf16 v[\\d:\\d+3],v[\\a:\\a+3],v[\\b:\\b+3],v[\\d:\\d+3]\n"
".endm\n"
".macro RD d,a,o\n"
" ds_read_b128 v[\\d:\\d+3],v\\a offset:\\o\n"
".endm\n"
".macro AD2 c\n"
" v_add_f32 v[\\c],v[\\c],v[\\c+2]\n"
" v_add_f32 v[\\c+1],v[\\c+1],v[\\c+3]\n"
".endm\n"
".macro CV d,a,b\n"
" v_cvt_pk_bf16_f32 v[\\d],v[\\a],v[\\b]\n"
".endm\n"
".macro ZR a,n\n"
" .set zi,\\a\n"
" .rept \\n\n"
" v_mov_b64 v[zi:zi+1],0\n"
" .set zi,zi+2\n"
" .endr\n"
".endm\n"
".macro SPIN lab,inc\n"
" s_cmp_eq_u32 s80,0\n"
" s_cselect_b32 s81,\\inc,0\n"
" s_add_u32 s79,s79,s81\n"
" s_add_u32 s80,s80,1\n"
" s_cmp_gt_u32 s80," STR(SPINCAP) "\n"
" s_cselect_b32 s77,1,s77\n"
" s_cmp_lg_u32 s77,0\n"
" s_cbranch_scc1 \\lab\n"
".endm\n"
".macro EXPORT\n"
" RD 144,255,0\n"
" RD 148,255,1024\n"
" s_waitcnt lgkmcnt(0)\n"
" buffer_store_dwordx4 v[144:147],v249,%3,s74 offen sc1\n"
" s_cmp_eq_u32 s64,3\n"
" s_cbranch_scc1 1f\n"
" buffer_store_dwordx4 v[148:151],v249,%3,s74 offen offset:1024 sc1\n"
"1:\n"
".endm\n"
" s_mov_b32 s56,0xCCCCCCCC\n"
" s_mov_b32 s57,0xCCCCCCCC\n"
" s_lshl_b32 s58,s64,11\n"
" s_add_u32 s59,s58,2048\n"
" s_and_b32 s59,s59,8191\n"
" s_xor_b32 s60,s58,4096\n"
" s_xor_b32 s61,s59,4096\n"
" s_lshr_b32 s63,s64,1\n"
" s_lshl_b32 s63,s63,10\n"
" s_and_b32 s82,s64,1\n"
" s_lshl_b32 s82,s82,2\n"
" s_add_u32 s63,s63,s82\n"
" s_mov_b32 s87,0x3e0293ee\n"
" ZR 168,16\n"
" ZR 216,16\n"
" v_mov_b32 v203,0\n"
" s_waitcnt vmcnt(0) lgkmcnt(0)\n"
"RL_loop:\n"
" v_add_u32 v160,s58,v249\n"
" v_add_u32 v161,s59,v249\n"
" v_add_u32 v162,s60,v249\n"
" v_add_u32 v163,s61,v249\n"
" s_add_u32 s84,s65,65536\n"
" v_mov_b64 v[204:205],-1\n"
" v_mov_b64 v[206:207],-1\n"
" s_add_u32 s82,s67,s69\n"
" s_add_u32 s82,s82,s58\n"
" s_mov_b32 s80,0\n"
MT(40)
" buffer_load_dwordx4 v[128:131],v254,%3,s68 offen sc1\n"
" buffer_load_dwordx4 v[132:135],v254,%3,s68 offen offset:1024 sc1\n"
" s_waitcnt vmcnt(0)\n"
"RL_retry:\n"
MT(42)
" ds_write_b128 v160,v[128:131]\n"
" SQ 168,128,0\n"
" ds_write_b128 v160,v[132:135] offset:1024\n"
" v_max3_u32 v200,v128,v129,v130\n"
" SQ 172,128,8\n"
" v_max3_u32 v201,v131,v132,v133\n"
" v_max3_u32 v200,v200,v134,v135\n"
" SQ 168,132,16\n"
" v_max_u32 v200,v200,v201\n"
" v_cmp_eq_u32 vcc,-1,v200\n"
" s_cbranch_vccnz RL_failm\n"
"RL_okm:\n"
" s_waitcnt lgkmcnt(0)\n"
" s_barrier\n"
" RD 136,161,0\n"
" RD 140,161,1024\n"
" SQ 172,132,24\n"
" RD 144,162,0\n"
" RD 148,162,1024\n"
" SQ 176,128,128\n"
" RD 152,163,0\n"
" RD 156,163,1024\n"
" SQ 180,128,136\n"
" s_mov_b64 vcc,s[56:57]\n"
" s_cmp_eq_u32 s78,63\n"
" SQ 176,132,144\n"
" v_readfirstlane_b32 s85,v203\n"
" SQ 180,132,152\n"
" s_waitcnt lgkmcnt(5)\n"
" SQ 168,136,32\n"
" SQ 172,136,40\n"
" s_waitcnt lgkmcnt(4)\n"
" SQ 168,140,48\n"
" .if " STR(OPT) "&4\n"
" buffer_store_dwordx4 v[204:207],v249,%3,s82 offen sc1\n"
" .endif\n"
" SQ 172,140,56\n"
" s_waitcnt lgkmcnt(3)\n"
" SQ 168,144,64\n"
" SQ 172,144,72\n"
" s_waitcnt lgkmcnt(0)\n"
" SQ 168,148,80\n"
" RD 160,253,2048\n"
" RD 164,253,6144\n"
" SQ 172,148,88\n"
" SQ 168,152,96\n"
" .if " STR(OPT) "&4\n"
" buffer_store_dwordx4 v[204:207],v249,%3,s82 offen offset:1024 sc1\n"
" .endif\n"
" SQ 168,156,112\n"
" SQ 172,152,104\n"
" SQ 172,156,120\n"
" SQ 176,136,160\n"
" AD2 168\n"
" SQ 180,136,168\n"
" RD 136,253,0\n"
" v_add_u32 v170,2304,v251\n"
" SQ 176,140,176\n"
" AD2 172\n"
" SQ 180,140,184\n"
" CV 168,168,172\n"
" CV 169,169,173\n"
" SQ 176,144,192\n"
" ds_write2_b32 v251,v168,v169 offset1:72\n"
" RD 140,253,4096\n"
" SQ 180,144,200\n"
" RD 144,253,1024\n"
" SQ 176,148,208\n"
" SQ 180,148,216\n"
" s_waitcnt lgkmcnt(2)\n"
" s_barrier\n"
" SQ 176,152,224\n"
" RD 148,253,5120\n"
" RD 200,252,0\n"
" SQ 176,156,240\n"
" RD 204,252,64\n"
" RD 208,252,128\n"
" SQ 180,152,232\n"
" RD 212,252,192\n"
" SQ 180,156,248\n"
MT(44)
" SV 184,128,0\n"
" AD2 176\n"
" SV 184,132,32\n"
" RD 152,253,3072\n"
" RD 156,253,7168\n"
" SV 188,128,8\n"
" AD2 180\n"
" SV 188,132,40\n"
" CV 176,176,180\n"
" CV 177,177,181\n"
" SV 192,128,16\n"
" ds_write2_b32 v170,v176,v177 offset1:72\n"
" v_add_u32 v183,s63,v249\n"
" SV 192,132,48\n"
" AD2 184\n"
" SV 196,128,24\n"
" AD2 188\n"
" SV 196,132,56\n"
" s_waitcnt lgkmcnt(0)\n"
" s_barrier\n"
" MZ 168,136,200\n"
" RD 128,252,2304\n"
" RD 132,252,2368\n"
" MZ 172,140,200\n"
" RD 136,252,2432\n"
" RD 140,252,2496\n"
" MA 168,144,204\n"
" CV 184,184,185\n"
" CV 188,188,189\n"
" MA 172,148,204\n"
" AD2 192\n"
" MA 168,160,208\n"
" AD2 196\n"
" MA 172,164,208\n"
" CV 192,192,193\n"
" CV 196,196,197\n"
" MA 168,152,212\n"
" ds_write_b32 v183,v184 offset:13312\n"
" ds_write_b32 v183,v188 offset:15360\n"
" MA 172,156,212\n"
" ds_write_b32 v183,v192 offset:13320\n"
" ds_write_b32 v183,v196 offset:15368\n"
" s_waitcnt lgkmcnt(4)\n"
" MZ 176,128,200\n"
" RD 144,253,8192\n"
" RD 148,253,9216\n"
" MA 176,132,204\n"
" RD 152,253,10240\n"
" RD 156,253,11264\n"
" MA 176,136,208\n"
" v_cndmask_b32 v168,v168,v172,vcc\n"
" v_cndmask_b32 v169,v169,v173,vcc\n"
" MA 176,140,212\n"
" v_cndmask_b32 v170,v170,v174,vcc\n"
" v_cndmask_b32 v171,v171,v175,vcc\n"
" v_max3_f32 v180,v168,v169,v170\n"
" v_max_f32 v180,v180,v171\n"
" s_waitcnt lgkmcnt(4)\n"
" s_barrier\n"
" v_max_f32_dpp v180,v180,v180 quad_perm:[2,3,0,1] row_mask:0xf bank_mask:0xf\n"
" s_cbranch_scc1 RL_last\n"
MT(46)
" v_max3_f32 v181,v180,v176,v177\n"
" v_max3_f32 v180,v180,v176,v177\n"
" v_cndmask_b32 v182,v176,v177,vcc\n"
" RD 196,216,18432\n"
" v_permlane16_swap_b32 v180,v181\n"
" v_max_f32 v183,v180,v181\n"
" v_max_f32 v180,v180,v181\n"
" RD 200,249,13312\n"
" RD 204,249,14336\n"
" v_permlane32_swap_b32 v180,v183\n"
" v_max_f32 v180,v180,v183\n"
" v_mul_f32 v180,s87,v180\n"
" v_fma_f32 v168,v168,s87,-v180\n"
" v_fma_f32 v169,v169,s87,-v180\n"
" v_exp_f32 v168,v168\n"
" v_fma_f32 v170,v170,s87,-v180\n"
" v_exp_f32 v169,v169\n"
" v_fma_f32 v171,v171,s87,-v180\n"
" v_exp_f32 v170,v170\n"
" v_exp_f32 v171,v171\n"
" CV 160,168,169\n"
" CV 161,170,171\n"
" v_cndmask_b32 v172,v160,v216,vcc\n"
" v_cndmask_b32 v173,v161,v216,vcc\n"
" v_cndmask_b32_dpp v174,v160,v216,vcc quad_perm:[2,3,0,1] row_mask:0xf bank_mask:0xf\n"
" v_cndmask_b32_dpp v175,v161,v216,vcc quad_perm:[2,3,0,1] row_mask:0xf bank_mask:0xf\n"
" v_fma_f32 v182,v182,s87,-v180\n"
" s_waitcnt lgkmcnt(0)\n"
" MZ 184,144,172\n"
" v_exp_f32 v182,v182\n"
" v_mov_b32_dpp v164,v172 quad_perm:[2,3,0,1] row_mask:0xf bank_mask:0xf\n"
" MZ 188,148,172\n"
" v_mov_b32_dpp v165,v173 quad_perm:[2,3,0,1] row_mask:0xf bank_mask:0xf\n"
" v_mov_b32_dpp v166,v174 quad_perm:[2,3,0,1] row_mask:0xf bank_mask:0xf\n"
" v_mov_b32_dpp v167,v175 quad_perm:[2,3,0,1] row_mask:0xf bank_mask:0xf\n"
" v_mov_b32_dpp v181,v182 quad_perm:[2,3,0,1] row_mask:0xf bank_mask:0xf\n"
" CV 162,182,181\n"
" MA 184,152,164\n"
" v_cndmask_b32 v168,v162,v216,vcc\n"
" v_cndmask_b32 v169,v162,v216,vcc\n"
" MZ 192,196,160\n"
" v_mov_b32_dpp v170,v168 quad_perm:[2,3,0,1] row_mask:0xf bank_mask:0xf\n"
" v_mov_b32_dpp v171,v168 quad_perm:[2,3,0,1] row_mask:0xf bank_mask:0xf\n"
" MA 188,156,164\n"
" RD 208,249,15360\n"
" RD 212,249,16384\n"
" MA 184,200,168\n"
" MA 184,204,168\n"
" v_add_f32_dpp v192,v192,v192 quad_perm:[2,3,0,1] row_mask:0xf bank_mask:0xf\n"
" s_waitcnt lgkmcnt(1)\n"
" MA 188,208,168\n"
" v_rcp_f32 v192,v192\n"
" s_waitcnt lgkmcnt(0)\n"
" MA 188,212,168\n"
" s_nop 3\n"
" v_mul_f32 v184,v184,v192\n"
" v_mul_f32 v185,v185,v192\n"
" v_mul_f32 v186,v186,v192\n"
" v_mul_f32 v187,v187,v192\n"
" v_mul_f32 v188,v188,v192\n"
" v_mul_f32 v189,v189,v192\n"
" v_mul_f32 v190,v190,v192\n"
" v_mul_f32 v191,v191,v192\n"
" CV 172,184,188\n"
" CV 173,185,189\n"
" CV 174,186,190\n"
" CV 175,187,191\n"
MT(48)
" s_nop 1\n"
" SV 216,172,64\n"
" SV 220,172,72\n"
" SV 224,172,80\n"
" SV 228,172,88\n"
" AD2 216\n"
" CV 128,216,217\n"
" SV 232,172,96\n"
" AD2 220\n"
" CV 129,220,221\n"
" SV 236,172,104\n"
" AD2 224\n"
" CV 130,224,225\n"
" SV 240,172,112\n"
" AD2 228\n"
" CV 131,228,229\n"
" SV 244,172,120\n"
" buffer_store_dwordx4 v[128:131],v250,%3,s65 offen sc1\n"
" AD2 232\n"
" AD2 236\n"
" AD2 240\n"
" CV 132,232,233\n"
" AD2 244\n"
" CV 133,236,237\n"
" CV 134,240,241\n"
" CV 135,244,245\n"
" buffer_store_dwordx4 v[132:135],v250,%3,s84 offen sc1\n"
MT(50)
" RD 160,249,17408\n"
" ZR 168,16\n"
" ZR 216,16\n"
" s_add_u32 s68,s68,16384\n"
" s_cmp_lg_u32 s64,0\n"
" s_cbranch_scc1 RL_other\n"
" s_add_u32 s82,s65,s69\n"
" s_add_u32 s83,s82,4096\n"
" s_lshr_b32 s84,s69,7\n"
" s_add_u32 s84,s84,s68\n"
" s_mov_b32 s80,0\n"
" s_waitcnt lgkmcnt(0)\n"
" s_sleep " STR(DL1) "\n"
MT(52)
"RL_red:\n"
" buffer_load_dwordx4 v[128:131],v249,%3,s82 offen sc1\n"
" buffer_load_dwordx4 v[132:135],v249,%3,s83 offen sc1\n"
" buffer_load_dwordx4 v[136:139],v249,%3,s82 offen offset:1024 sc1\n"
" buffer_load_dwordx4 v[140:143],v249,%3,s83 offen offset:1024 sc1\n"
" buffer_load_dwordx4 v[144:147],v249,%3,s82 offen offset:2048 sc1\n"
" buffer_load_dwordx4 v[148:151],v249,%3,s83 offen offset:2048 sc1\n"
" buffer_load_dwordx4 v[152:155],v249,%3,s82 offen offset:3072 sc1\n"
" buffer_load_dwordx4 v[156:159],v249,%3,s83 offen offset:3072 sc1\n"
" s_waitcnt vmcnt(7)\n"
" MZ 164,160,128\n"
" s_waitcnt vmcnt(6)\n"
" MA 164,160,132\n"
" s_waitcnt vmcnt(5)\n"
" MA 164,160,136\n"
" s_waitcnt vmcnt(4)\n"
" MA 164,160,140\n"
" s_waitcnt vmcnt(3)\n"
" MA 164,160,144\n"
" s_waitcnt vmcnt(2)\n"
" MA 164,160,148\n"
" s_waitcnt vmcnt(1)\n"
" MA 164,160,152\n"
" s_waitcnt vmcnt(0)\n"
" MA 164,160,156\n"
" s_nop 7\n"
" v_cmp_u_f32 vcc,v164,v164\n"
" v_add_f32_dpp v164,v164,v164 row_ror:8 row_mask:0xf bank_mask:0xf\n"
" v_add_f32_dpp v165,v165,v165 row_ror:8 row_mask:0xf bank_mask:0xf\n"
" v_add_f32_dpp v166,v166,v166 row_ror:8 row_mask:0xf bank_mask:0xf\n"
" v_add_f32_dpp v167,v167,v167 row_ror:8 row_mask:0xf bank_mask:0xf\n"
" s_cbranch_vccnz RL_failr\n"
"RL_okr:\n"
" s_mov_b64 exec,0x00FF00FF\n"
" CV 164,164,165\n"
" CV 165,166,167\n"
" buffer_store_dwordx2 v[164:165],v255,%3,s84 offen sc1\n"
" s_mov_b64 exec,-1\n"
"RL_join:\n"
MT(54)
" s_barrier\n"
" s_add_u32 s78,s78,1\n"
" s_mov_b32 s81,s67\n"
" s_mov_b32 s67,s65\n"
" s_mov_b32 s65,s66\n"
" s_mov_b32 s66,s81\n"
" v_xor_b32 v253,16384,v253\n"
MTACC
" s_sleep " STR(DL2) "\n"
" s_branch RL_loop\n"
"RL_other:\n"
" v_mov_b64 v[164:165],-1\n"
" v_mov_b64 v[166:167],-1\n"
" v_mov_b32 v202,s78\n"
" s_add_u32 s82,s67,s71\n"
" s_mov_b32 m0,s76\n"
" s_add_u32 s86,s78,2\n"
" s_min_u32 s86,s86,62\n"
" s_mov_b32 s80,0\n"
" s_cmp_lt_u32 s85,s86\n"
" s_cbranch_scc1 RL_failf\n"
"RL_okf:\n"
" s_xor_b32 s72,s72,128\n"
" s_sleep " STR(DL3) "\n"
" .if (" STR(OPT) "&1)==0\n"
" buffer_load_dwordx4 v249,%3,s70 offen sc1 lds\n"
" buffer_load_dwordx4 v249,%3,s70 offen offset:1024 sc1 lds\n"
" buffer_load_dwordx4 v249,%3,s70 offen offset:2048 sc1 lds\n"
" buffer_load_dwordx4 v249,%3,s70 offen offset:3072 sc1 lds\n"
" .endif\n"
" buffer_load_dword v203,off,%3,s72 sc1\n"
" s_sleep " STR(DL4) "\n"
" buffer_store_dword v202,off,%3,s73 sc1\n"
" .if (" STR(OPT) "&2)==0\n"
" EXPORT\n"
" .endif\n"
" .if (" STR(OPT) "&4)==0\n"
" buffer_store_dwordx4 v[164:167],v249,%3,s82 offen sc1\n"
" buffer_store_dwordx4 v[164:167],v249,%3,s82 offen offset:1024 sc1\n"
" buffer_store_dwordx4 v[164:167],v249,%3,s82 offen offset:2048 sc1\n"
" .endif\n"
" s_add_u32 s70,s70,0x80000\n"
" s_add_u32 s74,s74,s75\n"
" s_xor_b32 s76,s76,16384\n"
" s_branch RL_join\n"
"RL_failm:\n"
" SPIN RL_okm,1\n"
" buffer_load_dwordx4 v[128:131],v254,%3,s68 offen sc1\n"
" buffer_load_dwordx4 v[132:135],v254,%3,s68 offen offset:1024 sc1\n"
" ZR 168,4\n"
" s_waitcnt vmcnt(0)\n"
" s_branch RL_retry\n"
"RL_failr:\n"
" .set zi,129\n"
" .rept 15\n"
" v_max3_u32 v128,v128,v[zi],v[zi+1]\n"
" .set zi,zi+2\n"
" .endr\n"
" v_max_u32 v128,v128,v159\n"
" v_cmp_eq_u32 vcc,-1,v128\n"
" s_cbranch_vccz RL_okr\n"
" SPIN RL_okr,1024\n"
" s_branch RL_red\n"
"RL_failf:\n"
" SPIN RL_okf,0x100000\n"
" buffer_load_dword v200,off,%3,s72 sc1\n"
" s_waitcnt vmcnt(0)\n"
" v_readfirstlane_b32 s85,v200\n"
" s_nop 3\n"
" s_cmp_lt_u32 s85,s86\n"
" s_cbranch_scc1 RL_failf\n"
" s_branch RL_okf\n"
"RL_last:\n"
" s_cmp_eq_u32 s64,0\n"
" s_cbranch_scc1 2f\n"
" s_waitcnt lgkmcnt(0)\n"
" EXPORT\n"
"2:\n"
" v_mov_b32 v202,64\n"
" s_waitcnt vmcnt(0) lgkmcnt(0)\n"
" s_barrier\n"
" buffer_store_dword v202,off,%3,s73 sc1\n"
" s_nop 7\n"
  :"+{s[64:79]}"(sc),"+{v[248:255]}"(vc),"+{s[88:95]}"(pc)
  :"s"(rw),"{a[0:31]}"(wa[0]),"{a[32:63]}"(wa[1]),"{a[64:95]}"(wa[2]),"{a[96:127]}"(wa[3]),"{a[128:159]}"(wa[4]),"{a[160:191]}"(wa[5]),"{a[192:223]}"(wa[6]),"{a[224:255]}"(wa[7]),
   "{v[0:31]}"(wv[0]),"{v[32:63]}"(wv[1]),"{v[64:95]}"(wo[0]),"{v[96:127]}"(wo[1])
  :"memory","scc","vcc",CLOB);
  dead=sc[13];
  TS(3) TC(9,sc[15]&1023) TC(10,(sc[15]>>10)&1023)
}
DI u32x4 pk8(f32x4 a,f32x4 b){ return u32x4{pk(a.x,b.x),pk(a.y,b.y),pk(a.z,b.z),pk(a.w,b.w)}; }
DI u32x4 pkc(f32x4 a,f32x4 b){ return u32x4{pk(a.x,a.y),pk(a.z,a.w),pk(b.x,b.y),pk(b.z,b.w)}; }
DI void roleB1(const Args& A,char* ws,int h,int par,const int w,int& dead){
  const int l=lane(),g=l>>4,n=l&15;
  rsrc_t rw=mk(ws,WS_SZ); const u32 l16=l*16;
  enum{LA=3,L_B=65536,L_M=139264};
  UNR for(int i=0;i<8;i++) dma16(rw,l16,O_XF+par*32768+(w*8+i)*1024,(w*8+i)*1024);
  u32x4 wq[16][2],wk[16][2],wv[16][2];
  ldw<16,16>(wk,mk(A.wk,4*MB),g*65536+n*4,h*256+w*64,8192);
  ldw<16,10>(wv,mk(A.wv,4*MB),g*65536+n*8,h*256+(w>>1)*128+(w&1)*4,8192);
  ldw<16,0>(wq,mk(A.wq,4*MB),g*65536+n*4,h*256+w*64,8192);
  int bi=0; TS(1)
  for(int i=0;i<32+LA;i++){
    const int s=2*i+par,sa=s-2*LA; const u32 lb=L_B+bi*24576+l16; u32x4 pq[2],pk_[2],pv[2];
    u32 fl=ld1c(rw,(l&1)*128,O_FL+F_KM+2*h*128); vm0(); bar();
    if(i>LA&&w==0) st1c(rw,0,O_FL+F_O+(sa-2)*128+h*4,1);
    if(i>=LA){
      int sp=0; while(!dead&&__any(fl<(u32)sa+1)){ if(++sp>SPINCAP){dead=1;break;} __builtin_amdgcn_s_sleep(2); fl=ld1c(rw,(l&1)*128,O_FL+F_KM+2*h*128); }
      dma16(rw,(2*(n>>2)+(n&1))*256+g*16,O_KM+(sa*32+h)*2048+w*64,L_M+w*1024);
      UNR for(int j=0;j<2;j++) dma16(rw,l16,O_VM+(sa*32+h)*8192+(2*w+j)*1024,L_M+4096+(2*w+j)*1024);
    }
    if(i<32){
      if(i<31){ UNR for(int j=0;j<8;j++) dma16(rw,l16,O_XF+(s+2)*32768+(w*8+j)*1024,((i+1)&1)*32768+(w*8+j)*1024); }
      f32x4 cq[2][2]={},ck[2][2]={},cv[2][2]={};
      u32x4 xx[2][2]; const u32 xb=(i&1)*32768+l16; xx[0][0]=LDS(u32x4,xb); xx[0][1]=LDS(u32x4,xb+16384);
      UNR for(int ks=0;ks<16;ks++){ if(ks<15){ xx[(ks+1)&1][0]=LDS(u32x4,xb+(ks+1)*1024); xx[(ks+1)&1][1]=LDS(u32x4,xb+(ks+17)*1024); } SB();
        UNR for(int kt=0;kt<2;kt++){ u32x4 x=xx[ks&1][kt];
        UNR for(int t=0;t<2;t++){ ck[kt][t]=mfma(wk[ks][t],x,ck[kt][t]); cv[kt][t]=mfma(x,wv[ks][t],cv[kt][t]); cq[kt][t]=mfma(wq[ks][t],x,cq[kt][t]); } } SB(); }
      vm0(); const u32 gb=O_KV+(s*32+h)*16384;
      UNR for(int j=0;j<2;j++){ pk_[j]=pk8(ck[j][0],ck[j][1]); pv[j]=pkc(cv[0][j],cv[1][j]);
        st4c(rw,l16,gb+(j*4+w)*1024,pk_[j]); st4c(rw,l16,gb+8192+(2*w+j)*1024,pv[j]); }
      UNR for(int j=0;j<2;j++) pq[j]=pk8(cq[j][0],cq[j][1]);
    }
    if(i>=LA){
      const int qt=w&1,eh=w>>1; f32x4 s0={},s1={},sm={}; u32x4 qf[4],kf[4];
      UNR for(int k=0;k<4;k++){ qf[k]=LDS(u32x4,lb+16384+(qt*4+k)*1024); kf[k]=LDS(u32x4,lb+k*1024); } SB();
      UNR for(int k=0;k<4;k++){ s0=mfma(kf[k],qf[k],s0); SB(); kf[k]=LDS(u32x4,lb+(4+k)*1024); SB(); }
      UNR for(int k=0;k<4;k++) s1=mfma(kf[k],qf[k],s1);
      SB(); if(i>=32) vm0();
      bar();
      UNR for(int k=0;k<4;k++) kf[k]=LDS(u32x4,L_M+k*1024+l16); SB();
      UNR for(int k=0;k<4;k++){ sm=mfma(kf[k],qf[k],sm); SB(); kf[k]=LDS(u32x4,lb+8192+(eh*4+k)*1024); qf[k]=LDS(u32x4,L_M+4096+(eh*4+k)*1024+l16); SB(); }
      const int dq=8*(n>>2)+4*qt+(n&3)-8*g; float pe[10],mx=fmaxf(sm.x,sm.y)*CEXP;
      UNR for(int j=0;j<8;j++){ pe[j]=j<=dq?(j<4?s0[j&3]:s1[j&3])*CEXP:-1.4426950e9f; mx=fmaxf(mx,pe[j]); }
      pe[8]=sm.x*CEXP; pe[9]=sm.y*CEXP; mx=gmax(mx); float su=0;
      UNR for(int j=0;j<10;j++){ pe[j]=__builtin_amdgcn_exp2f(pe[j]-mx); su+=pe[j]; }
      su=__builtin_amdgcn_rcpf(gsum(su));
      u32x4 pf={pk(pe[0],pe[1]),pk(pe[2],pe[3]),pk(pe[4],pe[5]),pk(pe[6],pe[7])}; u32 p1=pk(pe[8],pe[9]); u32x4 pm={p1,p1,p1,p1};
      f32x4 d[4];
      UNR for(int t=0;t<4;t++){ d[t]=mfma(kf[t],pf,f32x4{0,0,0,0});
        d[t]=mfma(u32x4{qf[(t&1)*2][t&2],qf[(t&1)*2][(t&2)+1],qf[(t&1)*2+1][t&2],qf[(t&1)*2+1][(t&2)+1]},pm,d[t])*su; }
      SB(); vm0();
      UNR for(int j=0;j<2;j++) st4c(rw,l16,O_OF+((2*sa+qt)*128+h*4+eh*2+j)*1024,
        u32x4{pk(d[0][2*j],d[1][2*j]),pk(d[2][2*j],d[3][2*j]),pk(d[0][2*j+1],d[1][2*j+1]),pk(d[2][2*j+1],d[3][2*j+1])});
    } else vm0();
    if(i<32){ bar();
      if(w==0) st1c(rw,0,O_FL+F_KV+(2*h+par)*128,s+1);
      UNR for(int j=0;j<2;j++){ LDS(u32x4,lb+(j*4+w)*1024)=pk_[j]; LDS(u32x4,lb+8192+(2*w+j)*1024)=pv[j]; LDS(u32x4,lb+16384+(j*4+w)*1024)=pq[j]; } }
    bi=bi==LA-1?0:bi+1;
  }
  vm0(); bar();
  if(w==0) st1c(rw,0,O_FL+F_O+(62+par)*128+h*4,1);
}
DI float gelu(float x){ float u=x*(1.f+0.044715f*x*x)*-2.3022082f; return x*__builtin_amdgcn_rcpf(1.f+__builtin_amdgcn_exp2f(u)); }
DI void roleB2(const Args& A,char* ws,int grp,int j,const int w,int& dead){
  const int l=lane(),g=l>>4,n=l&15;
  rsrc_t rw=mk(ws,WS_SZ); const u32 l16=l*16;
  waitf(rw,O_FL+F_KV+(grp*16+j)*128,1,1,dead);
  u32x4 wo[32][2],wi[16][2];
  { rsrc_t ro=mk(A.wo,4*MB); u32 t[2][24];
    UNR for(int b=0;b<=11;b++){
      if(b<11){ UNR for(int i=0;i<24;i++){ int ks=b*3+i/8; if(ks<32) t[b&1][i]=ld1(ro,g*16384+n*4,j*64+(w*1024+(ks>>1)*64+(ks&1)*8+(i&7))*1024); } }
      SB();
      if(b>0){ UNR for(int q=0;q<3;q++){ int ks=(b-1)*3+q; if(ks<32){ u32x4 f0,f1; mkfr(&t[(b-1)&1][q*8],f0,f1);
        if(ks<30){ wo[ks][0]=toA(f0); wo[ks][1]=toA(f1);} else { wo[ks][0]=f0; wo[ks][1]=f1; } } } }
      SB(); } }
  const int un=j+16*w,isz2=un>=22,u=un-22*isz2;
  ldw<16,0>(wi,mk(A.win,512*1376*2),(un<44&&u*32+2*n<688)?g*8*2752+n*4:0x7FFFF000u,(un<44?isz2*1376+u*64:0),2752);
  TS(1)
  for(int blk=grp;blk<128;blk+=4){
    const int s=blk>>1,kt=blk&1,tk=s*32+8*(n>>2)+4*kt+(n&3);
    u32x4 xr; if(w==0) xr=((const u32x4*)A.x)[tk*64+j*4+g];
    waitf(rw,O_FL+F_O+s*128,32,1,dead);
    TS(2) f32x4 c0={},c1={}; u32x4 o[2][8];
    UNR for(int b=0;b<=4;b++){
      if(b<4){ UNR for(int i=0;i<8;i++) o[b&1][i]=ld4c(rw,l16+(i&3)*1024,O_OF+(blk*128+w*32+b*8+(i&4))*1024); }
      SB();
      if(b>0){ UNR for(int i=0;i<8;i++){ c0=mfma(wo[(b-1)*8+i][0],o[(b-1)&1][i],c0); c1=mfma(wo[(b-1)*8+i][1],o[(b-1)&1][i],c1); } }
      SB(); }
    if(w){ LDS(f32x4,w*2048+l16)=c0; LDS(f32x4,w*2048+1024+l16)=c1; }
    bar();
    if(w==0){ UNR for(int q=1;q<4;q++){ c0+=LDS(f32x4,q*2048+l16); c1+=LDS(f32x4,q*2048+1024+l16); }
      u32x4 a=pk8(c0,c1),hh; UNR for(int i=0;i<4;i++) hh[i]=pk(blo(a[i])+blo(xr[i]),bhi(a[i])+bhi(xr[i]));
      st4c(rw,l16,O_HF+(blk*16+j)*1024,hh); vm0(); st1c(rw,0,O_FL+F_H+blk*128+j*4,1); }
    waitf(rw,O_FL+F_H+blk*128,16,1,dead);
    TS(3) u32x4 hf[4]; float s1=0,s2=0,f[32];
    UNR for(int i=0;i<4;i++) hf[i]=ld4c(rw,l16+i*1024,O_HF+(blk*16+w*4)*1024);
    UNR for(int i=0;i<16;i++){ f[2*i]=blo(hf[i>>2][i&3]); f[2*i+1]=bhi(hf[i>>2][i&3]); s1+=f[2*i]+f[2*i+1]; }
    s1=gsum(s1)*(1.f/128); UNR for(int i=0;i<32;i++) s2+=(f[i]-s1)*(f[i]-s1);
    s2=gsum(s2); if(g==0) LDS(f32x2,8192+n*32+w*8)=f32x2{s1,s2};
    bar();
    { f32x4 a=LDS(f32x4,8192+n*32),b=LDS(f32x4,8192+n*32+16); float mu=(a.x+a.z+b.x+b.z)*0.25f;
      float d0=a.x-mu,d1=a.z-mu,d2=b.x-mu,d3=b.z-mu;
      float rs=rsqrtf((a.y+a.w+b.y+b.w+128.f*(d0*d0+d1*d1+d2*d2+d3*d3))*(1.f/512)+1e-5f);
      UNR for(int i=0;i<4;i++){ u32x4 v; UNR for(int q=0;q<4;q++) v[q]=pk((f[i*8+2*q]-mu)*rs,(f[i*8+2*q+1]-mu)*rs); LDS(u32x4,16384+(w*4+i)*1024+l16)=v; } }
    bar();
    if(un<44){ f32x4 z0={},z1={};
      u32x4 x[16]; UNR for(int k=0;k<16;k++) x[k]=LDS(u32x4,16384+k*1024+l16); SB();
      if(!isz2){ UNR for(int k=0;k<16;k++){ z0=mfma(wi[k][0],x[k],z0); z1=mfma(wi[k][1],x[k],z1); } }
      else     { UNR for(int k=0;k<16;k++){ z0=mfma(x[k],wi[k][0],z0); z1=mfma(x[k],wi[k][1],z1); } }
      UNR for(int i=0;i<4;i++){ z0[i]=gelu(z0[i]); z1[i]=gelu(z1[i]); }
      if(!isz2) st4c(rw,l16,O_Z1+(blk*22+u)*1024,pk8(z0,z1));
      else { u32x4 v=pkc(z0,z1); st2c(rw,l16+kt*8,O_Z2+(s*44+2*u)*1024,u32x2{v.x,v.y}); st2c(rw,l16+kt*8,O_Z2+(s*44+2*u+1)*1024,u32x2{v.z,v.w});
        f32x4 a,b; UNR for(int i=0;i<4;i++){ float p=(i&1)?bhi(v[i>>1]):blo(v[i>>1]),q=(i&1)?bhi(v[2+(i>>1)]):blo(v[2+(i>>1)]); a[i]=rsum16(p+q); b[i]=rsum16(p*p+q*q); }
        if(n<2) st4c(rw,n*128+g*32+kt*16,O_ST+(s*22+u)*256,__builtin_bit_cast(u32x4,n?b:a)); } }
    vm0(); bar();
    if(w==0) st1c(rw,0,O_FL+F_Z+s*128+(kt*16+j)*4,1);
  }
}
DI void roleB3(const Args& A,char* ws,int tb,const int w,int& dead){
  const int l=lane(),g=l>>4,n=l&15;
  rsrc_t rw=mk(ws,WS_SZ),rs_=mk(A.wsp,8*MB); const u32 l16=l*16;
  f32x4 c[12][2]={}; float cr[2]={0,0}; u32x4 z[12];
  const int tk=tb*32+8*(n>>2)+(n&3); const int nt=w<2?12:10,t0=w<2?12*w:10*w+4;
  for(int s=0;s<64;s++){
    u32x4 b0=ld4(rs_,tk*4096+g*16,s*64),b1=ld4(rs_,tk*4096+g*16,s*64+16384);
    waitf(rw,O_FL+F_Z+s*128,32,1,dead);
    TS(2) f32x4 st; { u32x4 t[6]; UNR for(int u=0;u<6;u++) t[u]=ld4c(rw,(u==5&&g>=2)?0x7FFFF000u:l16+(u&3)*1024,O_ST+s*5632+(u&4)*1024);
      UNR for(int i=0;i<12;i++) if(i<nt) z[i]=ld4c(rw,l16+(i&3)*1024,O_Z2+(s*44+t0+(i&12))*1024);
      SB(); st=__builtin_bit_cast(f32x4,t[0]); UNR for(int u=1;u<6;u++) st+=__builtin_bit_cast(f32x4,t[u]); }
    UNR for(int i=0;i<4;i++){ float a=gsum(st[i])*(1.f/688),o=dppf<0x128>(a); st[i]=n<8?a:rsqrtf(fmaxf(a-o*o,0.f)+1e-5f); }
    if(g==0) LDS(f32x4,w*256+l16)=st;
    f32x4 m[2],r[2]; UNR for(int q=0;q<2;q++){ m[q]=LDS(f32x4,w*256+g*32+q*16); r[q]=LDS(f32x4,w*256+128+g*32+q*16); }
    UNR for(int q=0;q<4;q++){ float ra=r[q>>1][(q&1)*2],rb=r[q>>1][(q&1)*2+1],ma=m[q>>1][(q&1)*2],mb=m[q>>1][(q&1)*2+1];
      b0[q]=pk(blo(b0[q])*ra,bhi(b0[q])*rb); b1[q]=pk(blo(b1[q])*ra,bhi(b1[q])*rb);
      cr[0]+=blo(b0[q])*ma+bhi(b0[q])*mb; cr[1]+=blo(b1[q])*ma+bhi(b1[q])*mb; }
    UNR for(int i=0;i<12;i++) if(i<nt){ c[i][0]=mfma(z[i],b0,c[i][0]); c[i][1]=mfma(z[i],b1,c[i][1]); }
  }
  UNR for(int kt=0;kt<2;kt++){ float bs=blo(((const unsigned short*)A.bsp)[tk+4*kt])-gsum(cr[kt]);
    UNR for(int i=0;i<6;i++) if(2*i<nt) z[i]=ld4c(rw,l16,O_Z1+((tb*2+kt)*22+(t0>>1)+i)*1024);
    UNR for(int i=0;i<6;i++) if(2*i<nt){ u32x4 v; UNR for(int q=0;q<4;q++) v[q]=pk(blo(z[i][q])*(c[2*i][kt][q]+bs),bhi(z[i][q])*(c[2*i+1][kt][q]+bs));
      st4c(rw,l16,O_UF+((tb*2+kt)*22+(t0>>1)+i)*1024,v); } }
  vm0(); st1c(rw,0,O_FL+F_U+tb*128+w*4,1);
}
DI void roleF(const Args& A,char* ws,int tb,int nh,const int w,int& dead){
  const int l=lane(),g=l>>4,n=l&15;
  rsrc_t rw=mk(ws,WS_SZ),ro=mk(A.wout,688*1024); const u32 l16=l*16;
  u32x4 wf[22][4];
  { u32x2 t[2][16];
    UNR for(int b=0;b<=11;b++){
      if(b<11){ UNR for(int i=0;i<16;i++) t[b&1][i]=ld2(ro,g*8192+n*8+(b*64+(i>>3)*32+(i&7))*1024,nh*512+w*128); }
      SB();
      if(b>0){ UNR for(int q=0;q<8;q++){ const u32x2* p=&t[(b-1)&1][(q>>2)*8]; int hi=(q>>1)&1; u32x4 f;
          UNR for(int i=0;i<4;i++) f[i]=(q&1)?phi(p[2*i][hi],p[2*i+1][hi]):plo(p[2*i][hi],p[2*i+1][hi]);
          if(b>7) PIN(f); wf[(b-1)*2+(q>>2)][q&3]=b<=7?toA(f):f; } }
      SB(); } }
  TS(5) waitf(rw,O_FL+F_U+tb*128,4,1,dead); TS(6)
  UNR for(int i=0;i<11;i++) dma16(rw,l16,O_UF+(tb*44+w*11+i)*1024,(w*11+i)*1024);
  u32x4 hf[2][2]; UNR for(int kt=0;kt<2;kt++) UNR for(int q=0;q<2;q++) hf[kt][q]=ld4c(rw,g*512+q*256+n*16,O_HF+((tb*2+kt)*16+8*nh+2*w)*1024);
  vm0(); bar();
  f32x4 c[2][4]={};
  u32x4 x[2][4]; UNR for(int i=0;i<4;i++) x[0][i]=LDS(u32x4,((i&1)*22+(i>>1))*1024+l16);
  UNR for(int k=0;k<11;k++){ if(k<10){ UNR for(int i=0;i<4;i++) x[(k+1)&1][i]=LDS(u32x4,((i&1)*22+2*k+2+(i>>1))*1024+l16); } SB();
    UNR for(int i=0;i<4;i++) UNR for(int t=0;t<4;t++) c[i&1][t]=mfma(wf[2*k+(i>>1)][t],x[k&1][i],c[i&1][t]); SB(); }
  UNR for(int kt=0;kt<2;kt++) UNR for(int q=0;q<2;q++){ u32x4 v;
    UNR for(int i=0;i<4;i++){ u32 a=pk(c[kt][(i&1)*2][2*q+(i>>1)],c[kt][(i&1)*2+1][2*q+(i>>1)]); v[i]=pk(blo(a)+blo(hf[kt][q][i]),bhi(a)+bhi(hf[kt][q][i])); }
    ((u32x4*)A.out)[(tb*32+8*(n>>2)+4*kt+(n&3))*64+nh*32+w*8+g*2+q]=v; }
}
__global__ void __launch_bounds__(256,1) main_kernel(Args A){
  __shared__ __attribute__((aligned(1024))) char smem[LDS_SZ];
  asm volatile(""::"v"(smem):"memory");
  char* ws=wsbase(A.ws); const int b=blockIdx.x,x=b&7,y=b>>3; int dead=0; const int w=__builtin_amdgcn_readfirstlane(threadIdx.x>>6);
  TS(0)
  if(y<16){
    if(x<4) roleR(A,ws,8*x+(y>>1),y&1,w,dead); else roleB1(A,ws,8*(x-4)+(y>>1),y&1,w,dead);
    TS(4) roleF(A,ws,8*x+(y>>1),y&1,w,dead);
  }
  else if(x<4) roleB3(A,ws,x*16+y-16,w,dead);
  else roleB2(A,ws,x-4,y-16,w,dead);
  TS(7)
}
extern "C" void kernel_launch(void* const* d_in,const int* in_sizes,int n_in,void* d_out,int out_size,void* d_ws,size_t ws_size,hipStream_t stream){
  Args A{d_in[0],d_in[1],d_in[2],d_in[3],d_in[4],d_in[5],d_in[8],d_in[9],d_in[10],d_in[11],d_out,d_ws};
  pre_kernel<<<256,256,0,stream>>>(A);
  main_kernel<<<256,256,0,stream>>>(A);
}
```

```cpp
#define PF 1
#include <hip/hip_runtime.h>
#include <hip/hip_bf16.h>
typedef unsigned u32;
typedef unsigned long long u64;
typedef __attribute__((ext_vector_type(2))) u32 u32x2;
typedef __attribute__((ext_vector_type(4))) u32 u32x4;
typedef __attribute__((ext_vector_type(8))) u32 u32x8;
typedef __attribute__((ext_vector_type(2))) float f32x2;
typedef __attribute__((ext_vector_type(4))) float f32x4;
typedef __attribute__((ext_vector_type(2))) __bf16 bfx2;
typedef __attribute__((ext_vector_type(8))) __bf16 bfx8;
typedef __amdgpu_buffer_rsrc_t rsrc_t;
#define DI __device__ __forceinline__
#define UNR _Pragma("unroll")
#define SB() __builtin_amdgcn_sched_barrier(0)
#define SENT 0xFFFFFFFFu
#define SPINCAP 400000
#define MB (1u<<20)
#define O_M   (3*MB)
#define O_FL  (4*MB)
#define F_SEL 0
#define F_KV  1024
#define F_KM  (F_KV+8192)
#define F_O   (F_KM+8192)
#define F_U   (F_O+16384)
#define F_FI  (F_U+8192)
#define FL_SZ 65536
#define O_XF  (5*MB)
#define O_HF  (7*MB)
#define O_Z1  (9*MB)
#define O_Z2  (12*MB)
#define O_UF  (15*MB)
#define O_ST  (18*MB)
#define O_KM  (20*MB)
#define O_KV  (24*MB)
#define O_VM  (56*MB)
#define O_OF  (72*MB)
#define WS_SZ (88*MB)
#define LDS_SZ (152*1024)

DI rsrc_t mk(const void* p,u32 n){ return __builtin_amdgcn_make_buffer_rsrc((void*)p,0,n,0x00020000); }
DI u32x4 ld4(rsrc_t r,u32 v,u32 s){ return __builtin_amdgcn_raw_buffer_load_b128(r,v,s,0); }
DI u32x2 ld2(rsrc_t r,u32 v,u32 s){ return __builtin_amdgcn_raw_buffer_load_b64(r,v,s,0); }
DI u32   ld1(rsrc_t r,u32 v,u32 s){ return __builtin_amdgcn_raw_buffer_load_b32(r,v,s,0); }
DI u32x4 ld4c(rsrc_t r,u32 v,u32 s){ return __builtin_amdgcn_raw_buffer_load_b128(r,v,s,16); }
DI u32x2 ld2c(rsrc_t r,u32 v,u32 s){ return __builtin_amdgcn_raw_buffer_load_b64(r,v,s,16); }
DI u32   ld1c(rsrc_t r,u32 v,u32 s){ return __builtin_amdgcn_raw_buffer_load_b32(r,v,s,16); }
DI void st4c(rsrc_t r,u32 v,u32 s,u32x4 d){ __builtin_amdgcn_raw_buffer_store_b128(d,r,v,s,16); }
DI void st2c(rsrc_t r,u32 v,u32 s,u32x2 d){ __builtin_amdgcn_raw_buffer_store_b64(d,r,v,s,16); }
DI void st1c(rsrc_t r,u32 v,u32 s,u32 d){ __builtin_amdgcn_raw_buffer_store_b32(d,r,v,s,16); }
DI void dma16(rsrc_t r,u32 v,u32 s,u32 lds){ asm volatile("s_mov_b32 m0, %3\n s_nop 0\n buffer_load_dwordx4 %0, %1, %2 offen sc1 lds"::"v"(v),"s"(r),"s"(s),"s"(lds):"memory"); }
#define SCX "sc1"
template<int N4> DI void dmaN(rsrc_t r,u32 v,u32 s,u32 lds){ asm volatile("s_mov_b32 m0, %3\n .rept %4\n s_nop 0\n .irp o,0,1024,2048,3072\n buffer_load_dwordx4 %1, %2, %0 offen offset:\\o " SCX " lds\n .endr\n s_add_u32 m0,m0,4096\n s_add_u32 %0,%0,4096\n .endr":"+s"(s):"v"(v),"s"(r),"s"(lds),"n"(N4):"memory","scc"); }
DI void vm0(){ asm volatile("s_waitcnt vmcnt(0)":::"memory"); }
DI void bar(){ asm volatile("s_waitcnt lgkmcnt(0)\n s_barrier":::"memory"); }
DI u32 pk(float a,float b){ f32x2 v={a,b}; return __builtin_bit_cast(u32,__builtin_convertvector(v,bfx2)); }
DI float blo(u32 v){ return __uint_as_float(v<<16); }
DI float bhi(u32 v){ return __uint_as_float(v&0xFFFF0000u); }
DI u32 plo(u32 a,u32 b){ return __builtin_amdgcn_perm(b,a,0x05040100u); }
DI u32 phi(u32 a,u32 b){ return __builtin_amdgcn_perm(b,a,0x07060302u); }
DI f32x4 mfma(u32x4 a,u32x4 b,f32x4 c){ return __builtin_amdgcn_mfma_f32_16x16x32_bf16(__builtin_bit_cast(bfx8,a),__builtin_bit_cast(bfx8,b),c,0,0,0); }
DI float gmax(float x){ u32 u=__float_as_uint(x); auto a=__builtin_amdgcn_permlane16_swap(u,u,false,false); x=fmaxf(__uint_as_float(a[0]),__uint_as_float(a[1]));
  u=__float_as_uint(x); auto b=__builtin_amdgcn_permlane32_swap(u,u,false,false); return fmaxf(__uint_as_float(b[0]),__uint_as_float(b[1])); }
DI float gsum(float x){ u32 u=__float_as_uint(x); auto a=__builtin_amdgcn_permlane16_swap(u,u,false,false); x=__uint_as_float(a[0])+__uint_as_float(a[1]);
  u=__float_as_uint(x); auto b=__builtin_amdgcn_permlane32_swap(u,u,false,false); return __uint_as_float(b[0])+__uint_as_float(b[1]); }
template<int C> DI float dppf(float x){ return __int_as_float(__builtin_amdgcn_update_dpp(0,__float_as_int(x),C,0xF,0xF,true)); }
DI float rsum16(float x){ x+=dppf<0x128>(x); x+=dppf<0x124>(x); x+=dppf<0x122>(x); x+=dppf<0x121>(x); return x; }
template<class T> DI T toA(T v){ T o; asm("":"=a"(o):"0"(v)); return o; }
#define LDS(T,off) (*(__attribute__((address_space(3))) T*)(size_t)(off))
DI int lane(){ int l; asm volatile("v_mbcnt_lo_u32_b32 %0, -1, 0\n v_mbcnt_hi_u32_b32 %0, -1, %0":"=v"(l)); __builtin_assume(l>=0&&l<64); return l; }
DI void waitf(rsrc_t r,u32 off,u32 n,u32 need,int& dead,u32 str=4,u32 vo=0){
  if(n>1&&str) vo=(lane()&(n-1))*str; int sp=0;
  while(!dead){ u32 v=ld1c(r,vo,off); if(!__any(v<need)) break; if(++sp>SPINCAP){dead=1;break;} __builtin_amdgcn_s_sleep(2); }
  asm volatile("":::"memory");
}
DI void mkfr(const u32* t,u32x4& f0,u32x4& f1){
  f0=u32x4{plo(t[0],t[1]),plo(t[2],t[3]),plo(t[4],t[5]),plo(t[6],t[7])};
  f1=u32x4{phi(t[0],t[1]),phi(t[2],t[3]),phi(t[4],t[5]),phi(t[6],t[7])};
}
DI void dmaw(rsrc_t r,u32 v,u32 s,u32 lds){ asm volatile("s_mov_b32 m0, %3\n s_nop 0\n buffer_load_dwordx4 %0, %1, %2 offen lds"::"v"(v),"s"(r),"s"(s),"s"(lds):"memory"); }
template<int NC,int NR,class I,class U> DI void stream(u32 base,I issue,U use){
  UNR for(int c=0;c<NR;c++) issue(c,base+c*4096);
  UNR for(int c=0;c<NC;c++){ asm volatile("s_waitcnt vmcnt(%0)"::"n"(4*((NC-c<NR?NC-c:NR)-1)):"memory");
    use(c,(c%NR)*4096); SB(); asm volatile("s_waitcnt lgkmcnt(0)":::"memory");
    if(c+NR<NC) issue(c+NR,base+(c%NR)*4096); SB(); }
}
template<int NKS,int NA> DI void ldw(u32x4 (*w)[2],rsrc_t r,u32 vo,u32 so,u32 rs){
  u32 t[2][24];
  constexpr int NB=(NKS+2)/3;
  UNR for(int b=0;b<=NB;b++){
    if(b<NB){ asm volatile("":"+s"(so)); UNR for(int i=0;i<24;i++) if(b*3+i/8<NKS) t[b&1][i]=ld1(r,vo,so+((b*3+i/8)*32+(i&7))*rs); }
    SB();
    if(b>0){ UNR for(int q=0;q<3;q++){ int ks=(b-1)*3+q; if(ks<NKS){ u32x4 f0,f1; mkfr(&t[(b-1)&1][q*8],f0,f1);
      if(ks<NA){ w[ks][0]=toA(f0); w[ks][1]=toA(f1);} else { w[ks][0]=f0; w[ks][1]=f1; } } } }
    SB();
  }
}
#define TC(i,v)
#define CNT(c,v)
#define PT(i)
#define PT0
#define PTE
#define TS(i) {}
#undef PT
#undef PT0
#undef PTE
#define PT(i)
#define PT0
#define PTE
struct Args{ const void *x,*mem0,*wq,*wk,*wv,*wo,*win,*wsp,*bsp,*wout; void *out,*ws; };
DI char* wsbase(void* ws){ return (char*)(((size_t)ws+(2*MB-1))&~(size_t)(2*MB-1)); }

__global__ void __launch_bounds__(256) pre_kernel(Args A){
  char* ws=wsbase(A.ws); rsrc_t rw=mk(ws,WS_SZ);
  int b=blockIdx.x,t=threadIdx.x,l=t&63,w=t>>6;
  u32x4 xv[2];
  UNR for(int i=0;i<2;i++) xv[i]=((const u32x4*)A.x)[(b*8+w*2+i)*64+l];
  u32x4 pf[16]; { const void* wp=(b>>6)==0?A.wk:(b>>6)==1?A.wv:(b>>6)==2?A.wq:A.wo; UNR for(int i=0;i<16;i++) pf[i]=((const u32x4*)wp)[(b&63)*4096+i*256+t]; }
  if((b&127)==0){
    if(w==0){
      if(l<8) st1c(rw,l*2048,b*16384,SENT);
      vm0(); u32 mn[2]={~0u,~0u};
      UNR for(int i=0;i<8;i++){ int j=(i>>1)+(i&1)*4;
        u64 c0=__builtin_amdgcn_s_memtime(); asm volatile("s_waitcnt lgkmcnt(0)":::"memory");
        u32 v=ld1c(rw,0,b*16384+j*2048); asm volatile("s_waitcnt vmcnt(0)":"+v"(v)::"memory");
        u64 c1=__builtin_amdgcn_s_memtime(); mn[i&1]=min(mn[i&1],(u32)(c1-c0)); }
      if(l==0) *(u32*)(ws+O_FL+F_SEL+(b>>7)*4)=mn[1]<mn[0]?8192u:0u;
    }
    __syncthreads();
  }
  UNR for(int i=0;i<4;i++){ u32x4 v={SENT,SENT,SENT,SENT}; int c=i*256+t;
    if(b==192){ int cc=c&511; const u32x2* p=(const u32x2*)A.mem0+((cc>>2)&7)*128+(cc>>6)*16+(cc&3)*4+((cc>>5)&1); u32x2 a=p[0],e=p[2]; v=u32x4{a.x,a.y,e.x,e.y}; }
    ((u32x4*)ws)[b*1024+c]=v; }
  if(b>=4&&t<16) ((u32x4*)(ws+O_FL))[b*16+t]=u32x4{0,0,0,0};
  UNR for(int i=0;i<2;i++){
    float f[8],s=0; UNR for(int j=0;j<4;j++){ f[2*j]=blo(xv[i][j]); f[2*j+1]=bhi(xv[i][j]); s+=f[2*j]+f[2*j+1]; }
    float mu=gsum(rsum16(s))*(1.f/512),q=0; UNR for(int j=0;j<8;j++){ f[j]-=mu; q+=f[j]*f[j]; }
    float rs=rsqrtf(gsum(rsum16(q))*(1.f/512)+1e-5f);
    int tk=b*8+w*2+i,tau=tk&31,kt=(tau>>2)&1,c=4*(tau>>3)+(tau&3);
    ((u32x4*)(ws+O_XF))[(((tk>>5)*2+kt)*16+(l>>2))*64+(l&3)*16+c]=u32x4{pk(f[0]*rs,f[1]*rs),pk(f[2]*rs,f[3]*rs),pk(f[4]*rs,f[5]*rs),pk(f[6]*rs,f[7]*rs)};
  }
  UNR for(int i=0;i<16;i++) asm volatile(""::"v"(pf[i]));
}
#define PIN(x) asm volatile("":"+v"(x))
#define DL1 8
#define DL2 7
#define DL3 9
#define DL4 0
#define OPT 4
#define STR_(x) #x
#define STR(x) STR_(x)
#define CEXP 0.12751743f
typedef __attribute__((ext_vector_type(16))) u32 u32x16;
typedef __attribute__((ext_vector_type(32))) u32 u32x32;
#define MT(i)
#define MTACC
#define CLOB "v128","v129","v130","v131","v132","v133","v134","v135","v136","v137","v138","v139","v140","v141","v142","v143","v144","v145","v146","v147","v148","v149","v150","v151","v152","v153","v154","v155","v156","v157","v158","v159","v160","v161","v162","v163","v164","v165","v166","v167","v168","v169","v170","v171","v172","v173","v174","v175","v176","v177","v178","v179","v180","v181","v182","v183","v184","v185","v186","v187","v188","v189","v190","v191","v192","v193","v194","v195","v196","v197","v198","v199","v200","v201","v202","v203","v204","v205","v206","v207","v208","v209","v210","v211","v212","v213","v214","v215","v216","v217","v218","v219","v220","v221","v222","v223","v224","v225","v226","v227","v228","v229","v230","v231","v232","v233","v234","v235","v236","v237","v238","v239","v240","v241","v242","v243","v244","v245","v246","v247","s40","s41","s42","s43","s44","s45","s46","s47","s48","s49","s50","s51","s52","s53","s54","s55","s56","s57","s58","s59","s60","s61","s62","s63","s80","s81","s82","s83","s84","s85","s86","s87"
DI void roleR(const Args& A,char* ws,int h,int th,const int w,int& dead){
  const int l=lane(),g=l>>4,n=l&15;
  rsrc_t rw=mk(ws,WS_SZ);
  u32x32 wa[8],wv[2],wo[2];
  { rsrc_t rq=mk(A.wq,4*MB),rk=mk(A.wk,4*MB),rv=mk(A.wv,4*MB),ro=mk(A.wo,4*MB);
    const u32 rl=(32*(g>>1)+8*(g&1))*8192+h*256;
#define ROWJ(j) ((16*(((j)>>1)&1)+4*(((j)>>3)&1)+2*(((j)>>2)&1)+((j)&1))*8192)
    u32x2 v[32]; u32x4 u[16];
    UNR for(int j=0;j<32;j++) v[j]=ld2(rv,rl+n*8,(2*w+(j>>4))*64*8192+th*128+ROWJ(j));
    UNR for(int j=0;j<16;j++) u[j]=ld4(ro,g*8192+n*16,(h*128+th*64+(j&7)+32*(j>>3))*1024+w*256);
    const u32 base=w*32768,va=ROWJ(g)+(32*(n>>3)+8*((n>>2)&1))*8192+h*256+(n&3)*16; u32 ra=base+l*4; PIN(ra); u32x32 f;
    stream<16,8>(base,[&](int c,u32 d){ u32 so=((c+2*w)&7)*64*8192+w*64; asm volatile("":"+s"(so),"+s"(d));
        UNR for(int i=0;i<4;i++) dmaw(c<8?rq:rk,va,so+ROWJ(4*i),d+i*1024); },
      [&](int c,u32 o){ u32 p[16]; UNR for(int i=0;i<16;i++) p[i]=LDS(u32,ra+o+i*256);
        UNR for(int j=0;j<16;j++){ int q=(j&7)*2; f[(c&1)*16+j]=(j&8)?phi(p[q],p[q+1]):plo(p[q],p[q+1]); }
#define PA(i,r) if(c==2*i+1) asm volatile("":"={a[" r "]}"(wa[i]):"0"(f));
        PA(0,"0:31") PA(1,"32:63") PA(2,"64:95") PA(3,"96:127") PA(4,"128:159") PA(5,"160:191") PA(6,"192:223") PA(7,"224:255") });
    bar();
    UNR for(int q=0;q<8;q++) UNR for(int j=0;j<8;j++){ const u32x2* p=v+(q>>2)*16+2*j; int hi=(q>>1)&1;
      wv[q>>2][(q&3)*8+j]=(q&1)?phi(p[0][hi],p[1][hi]):plo(p[0][hi],p[1][hi]); }
    asm volatile("":"+{v[0:31]}"(wv[0]),"+{v[32:63]}"(wv[1])); SB();
    UNR for(int q=0;q<8;q++) UNR for(int j=0;j<8;j++) wo[q>>2][(q&3)*8+j]=(q&1)?phi(u[2*j][q>>1],u[2*j+1][q>>1]):plo(u[2*j][q>>1],u[2*j+1][q>>1]);
    asm volatile("":"+{v[64:95]}"(wo[0]),"+{v[96:127]}"(wo[1]));
  }
  const u32 sel0=__builtin_amdgcn_readfirstlane(ld1(rw,0,O_FL+F_SEL)),sel1=__builtin_amdgcn_readfirstlane(ld1(rw,0,O_FL+F_SEL+4));
  const int pi=2*h+th,p=(n>>1)&1; const u32 l16=l*16;
  { u32x4 sel; UNR for(int j=0;j<4;j++) sel[j]=(n&3)==j?((n&4)?0x3F800000u:0x3F80u):0u; LDS(u32x4,17408+l16)=sel; LDS(u32x4,18432)=u32x4{0x3F803F80u,0x3F803F80u,0x3F80u,0}; }
  const int rho=(2*w+(n>>3))*8+g;
  u32x8 vc={p?0xEEEEu:0x4444u, l16, rho*16384+((pi+rho+(rho&3))&63)*128+(n&7)*16u, 8192+2*g*288+w*64+n*4u,
            8192+(2*(n>>2)+(n&1))*288+g*16u, 32768+l16, w*2048+((g>>1)*4+(n>>2))*128+(n&1)*64+(2*(g&1)+p)*16u,
            w==0?g*64+n*8u:w==3?10496+(4*th+g)*288+n*16u:13312+(w-1)*2048+l16};
  const u32 kvs=O_KV+h*16384+w*3072;
  TS(1) waitf(rw,O_FL+F_KV+2*h*128,1,1,dead); TS(2)
  UNR for(int i=0;i<3;i++) dma16(rw,l16,kvs+i*1024+(3*w+i>=8?th*4096:0),32768+(3*w+i)*1024);
  u32x16 sc={(u32)w, sel0, MB+sel0, 2*MB+sel1, O_M+sel1, pi*16384u, O_KV+h*16384+MB/2+(w-1+(w==3?th:0))*4096, pi*16384u+(w==3?5120:(w-1)*3072),
             O_FL+F_KV+(2*h+1)*128u, O_FL+F_KM+pi*128u, w==3?O_KM+h*2048+th*1024:O_VM+pi*4096+(w-1)*2048u, w==3?65536u:262144u,
             49152+(w-1)*4096u, (u32)dead, 0, 0};
  u32x8 pc={};
  asm volatile(
".macro SQ c,a,w\n"
" v_smfmac_f32_16x16x64_bf16 v[\\c:\\c+3],v[\\a:\\a+3],a[\\w:\\w+7],v248\n"
".endm\n"
".macro SV c,a,w\n"
" v_smfmac_f32_16x16x64_bf16 v[\\c:\\c+3],v[\\a:\\a+3],v[\\w:\\w+7],v248\n"
".endm\n"
".macro MZ d,a,b\n"
" v_mfma_f32_16x16x32_bf16 v[\\d:\\d+3],v[\\a:\\a+3],v[\\b:\\b+3],0\n"
".endm\n"
".macro MA d,a,b\n"
" v_mfma_f32_16x16x32_bf16 v[\\d:\\d+3],v[\\a:\\a+3],v[\\b:\\b+3],v[\\d:\\d+3]\n"
".endm\n"
".macro RD d,a,o\n"
" ds_read_b128 v[\\d:\\d+3],v\\a offset:\\o\n"
".endm\n"
".macro AD2 c\n"
" v_add_f32 v[\\c],v[\\c],v[\\c+2]\n"
" v_add_f32 v[\\c+1],v[\\c+1],v[\\c+3]\n"
".endm\n"
".macro CV d,a,b\n"
" v_cvt_pk_bf16_f32 v[\\d],v[\\a],v[\\b]\n"
".endm\n"
".macro ZR a,n\n"
" .set zi,\\a\n"
" .rept \\n\n"
" v_mov_b64 v[zi:zi+1],0\n"
" .set zi,zi+2\n"
" .endr\n"
".endm\n"
".macro SPIN lab,inc\n"
" s_cmp_eq_u32 s80,0\n"
" s_cselect_b32 s81,\\inc,0\n"
" s_add_u32 s79,s79,s81\n"
" s_add_u32 s80,s80,1\n"
" s_cmp_gt_u32 s80," STR(SPINCAP) "\n"
" s_cselect_b32 s77,1,s77\n"
" s_cmp_lg_u32 s77,0\n"
" s_cbranch_scc1 \\lab\n"
".endm\n"
".macro EXPORT\n"
" RD 144,255,0\n"
" RD 148,255,1024\n"
" s_waitcnt lgkmcnt(0)\n"
" buffer_store_dwordx4 v[144:147],v249,%3,s74 offen sc1\n"
" s_cmp_eq_u32 s64,3\n"
" s_cbranch_scc1 1f\n"
" buffer_store_dwordx4 v[148:151],v249,%3,s74 offen offset:1024 sc1\n"
"1:\n"
".endm\n"
" s_mov_b32 s56,0xCCCCCCCC\n"
" s_mov_b32 s57,0xCCCCCCCC\n"
" s_lshl_b32 s58,s64,11\n"
" s_add_u32 s59,s58,2048\n"
" s_and_b32 s59,s59,8191\n"
" s_xor_b32 s60,s58,4096\n"
" s_xor_b32 s61,s59,4096\n"
" s_lshr_b32 s63,s64,1\n"
" s_lshl_b32 s63,s63,10\n"
" s_and_b32 s82,s64,1\n"
" s_lshl_b32 s82,s82,2\n"
" s_add_u32 s63,s63,s82\n"
" s_mov_b32 s87,0x3e0293ee\n"
" ZR 168,16\n"
" ZR 216,16\n"
" v_mov_b32 v203,0\n"
" s_waitcnt vmcnt(0) lgkmcnt(0)\n"
"RL_loop:\n"
" v_add_u32 v160,s58,v249\n"
" v_add_u32 v161,s59,v249\n"
" v_add_u32 v162,s60,v249\n"
" v_add_u32 v163,s61,v249\n"
" s_add_u32 s84,s65,65536\n"
" v_mov_b32 v202,s78\n"
" v_mov_b64 v[204:205],-1\n"
" v_mov_b64 v[206:207],-1\n"
" s_add_u32 s82,s67,s69\n"
" s_add_u32 s82,s82,s58\n"
" s_mov_b32 s80,0\n"
MT(40)
" buffer_load_dwordx4 v[128:131],v254,%3,s68 offen sc1\n"
" buffer_load_dwordx4 v[132:135],v254,%3,s68 offen offset:1024 sc1\n"
" s_waitcnt vmcnt(0)\n"
"RL_retry:\n"
MT(42)
" ds_write_b128 v160,v[128:131]\n"
" SQ 168,128,0\n"
" ds_write_b128 v160,v[132:135] offset:1024\n"
" v_max3_u32 v200,v128,v129,v130\n"
" SQ 172,128,8\n"
" v_max3_u32 v201,v131,v132,v133\n"
" v_max3_u32 v200,v200,v134,v135\n"
" SQ 168,132,16\n"
" v_max_u32 v200,v200,v201\n"
" v_cmp_eq_u32 vcc,-1,v200\n"
" s_cbranch_vccnz RL_failm\n"
"RL_okm:\n"
" s_waitcnt lgkmcnt(0)\n"
" s_barrier\n"
" RD 136,161,0\n"
" RD 140,161,1024\n"
" SQ 172,132,24\n"
" RD 144,162,0\n"
" RD 148,162,1024\n"
" SQ 176,128,128\n"
" s_cmp_lg_u32 s64,3\n"
" RD 152,163,0\n"
" RD 156,163,1024\n"
" SQ 180,128,136\n"
" s_cbranch_scc1 3f\n"
" buffer_store_dword v202,off,%3,s73 sc1\n"
"3:\n"
" s_mov_b64 vcc,s[56:57]\n"
" s_cmp_eq_u32 s78,63\n"
" SQ 176,132,144\n"
" v_readfirstlane_b32 s85,v203\n"
" SQ 180,132,152\n"
" s_waitcnt lgkmcnt(5)\n"
" SQ 168,136,32\n"
" SQ 172,136,40\n"
" s_waitcnt lgkmcnt(4)\n"
" SQ 168,140,48\n"
" .if " STR(OPT) "&4\n"
" buffer_store_dwordx4 v[204:207],v249,%3,s82 offen sc1\n"
" .endif\n"
" SQ 172,140,56\n"
" s_waitcnt lgkmcnt(3)\n"
" SQ 168,144,64\n"
" SQ 172,144,72\n"
" s_waitcnt lgkmcnt(0)\n"
" SQ 168,148,80\n"
" RD 160,253,2048\n"
" RD 164,253,6144\n"
" SQ 172,148,88\n"
" SQ 168,152,96\n"
" .if " STR(OPT) "&4\n"
" buffer_store_dwordx4 v[204:207],v249,%3,s82 offen offset:1024 sc1\n"
" .endif\n"
" SQ 168,156,112\n"
" SQ 172,152,104\n"
" SQ 172,156,120\n"
" SQ 176,136,160\n"
" AD2 168\n"
" SQ 180,136,168\n"
" RD 136,253,0\n"
" v_add_u32 v170,2304,v251\n"
" SQ 176,140,176\n"
" AD2 172\n"
" SQ 180,140,184\n"
" CV 168,168,172\n"
" CV 169,169,173\n"
" SQ 176,144,192\n"
" ds_write2_b32 v251,v168,v169 offset1:72\n"
" RD 140,253,4096\n"
" SQ 180,144,200\n"
" RD 144,253,1024\n"
" SQ 176,148,208\n"
" SQ 180,148,216\n"
" s_waitcnt lgkmcnt(2)\n"
" s_barrier\n"
" SQ 176,152,224\n"
" RD 148,253,5120\n"
" RD 200,252,0\n"
" SQ 176,156,240\n"
" RD 204,252,64\n"
" RD 208,252,128\n"
" SQ 180,152,232\n"
" RD 212,252,192\n"
" SQ 180,156,248\n"
MT(44)
" SV 184,128,0\n"
" AD2 176\n"
" SV 184,132,32\n"
" RD 152,253,3072\n"
" RD 156,253,7168\n"
" SV 188,128,8\n"
" AD2 180\n"
" SV 188,132,40\n"
" CV 176,176,180\n"
" CV 177,177,181\n"
" SV 192,128,16\n"
" ds_write2_b32 v170,v176,v177 offset1:72\n"
" v_add_u32 v183,s63,v249\n"
" SV 192,132,48\n"
" AD2 184\n"
" SV 196,128,24\n"
" AD2 188\n"
" SV 196,132,56\n"
" s_waitcnt lgkmcnt(0)\n"
" s_barrier\n"
" MZ 168,136,200\n"
" RD 128,252,2304\n"
" RD 132,252,2368\n"
" MZ 172,140,200\n"
" RD 136,252,2432\n"
" RD 140,252,2496\n"
" MA 168,144,204\n"
" CV 184,184,185\n"
" CV 188,188,189\n"
" MA 172,148,204\n"
" AD2 192\n"
" MA 168,160,208\n"
" AD2 196\n"
" MA 172,164,208\n"
" CV 192,192,193\n"
" CV 196,196,197\n"
" MA 168,152,212\n"
" ds_write_b32 v183,v184 offset:13312\n"
" ds_write_b32 v183,v188 offset:15360\n"
" MA 172,156,212\n"
" ds_write_b32 v183,v192 offset:13320\n"
" ds_write_b32 v183,v196 offset:15368\n"
" s_waitcnt lgkmcnt(4)\n"
" MZ 176,128,200\n"
" RD 144,253,8192\n"
" RD 148,253,9216\n"
" MA 176,132,204\n"
" RD 152,253,10240\n"
" RD 156,253,11264\n"
" MA 176,136,208\n"
" v_cndmask_b32 v168,v168,v172,vcc\n"
" v_cndmask_b32 v169,v169,v173,vcc\n"
" MA 176,140,212\n"
" v_cndmask_b32 v170,v170,v174,vcc\n"
" v_cndmask_b32 v171,v171,v175,vcc\n"
" v_max3_f32 v180,v168,v169,v170\n"
" v_max_f32 v180,v180,v171\n"
" s_waitcnt lgkmcnt(4)\n"
" s_barrier\n"
" v_max_f32_dpp v180,v180,v180 quad_perm:[2,3,0,1] row_mask:0xf bank_mask:0xf\n"
" s_cbranch_scc1 RL_last\n"
MT(46)
" v_max3_f32 v181,v180,v176,v177\n"
" v_max3_f32 v180,v180,v176,v177\n"
" v_cndmask_b32 v182,v176,v177,vcc\n"
" RD 196,216,18432\n"
" v_permlane16_swap_b32 v180,v181\n"
" v_max_f32 v183,v180,v181\n"
" v_max_f32 v180,v180,v181\n"
" RD 200,249,13312\n"
" RD 204,249,14336\n"
" v_permlane32_swap_b32 v180,v183\n"
" v_max_f32 v180,v180,v183\n"
" v_mul_f32 v180,s87,v180\n"
" v_fma_f32 v168,v168,s87,-v180\n"
" v_fma_f32 v169,v169,s87,-v180\n"
" v_exp_f32 v168,v168\n"
" v_fma_f32 v170,v170,s87,-v180\n"
" v_exp_f32 v169,v169\n"
" v_fma_f32 v171,v171,s87,-v180\n"
" v_exp_f32 v170,v170\n"
" v_exp_f32 v171,v171\n"
" CV 160,168,169\n"
" CV 161,170,171\n"
" v_cndmask_b32 v172,v160,v216,vcc\n"
" v_cndmask_b32 v173,v161,v216,vcc\n"
" v_cndmask_b32_dpp v174,v160,v216,vcc quad_perm:[2,3,0,1] row_mask:0xf bank_mask:0xf\n"
" v_cndmask_b32_dpp v175,v161,v216,vcc quad_perm:[2,3,0,1] row_mask:0xf bank_mask:0xf\n"
" v_fma_f32 v182,v182,s87,-v180\n"
" s_waitcnt lgkmcnt(0)\n"
" MZ 184,144,172\n"
" v_exp_f32 v182,v182\n"
" v_mov_b32_dpp v164,v172 quad_perm:[2,3,0,1] row_mask:0xf bank_mask:0xf\n"
" MZ 188,148,172\n"
" v_mov_b32_dpp v165,v173 quad_perm:[2,3,0,1] row_mask:0xf bank_mask:0xf\n"
" v_mov_b32_dpp v166,v174 quad_perm:[2,3,0,1] row_mask:0xf bank_mask:0xf\n"
" v_mov_b32_dpp v167,v175 quad_perm:[2,3,0,1] row_mask:0xf bank_mask:0xf\n"
" v_mov_b32_dpp v181,v182 quad_perm:[2,3,0,1] row_mask:0xf bank_mask:0xf\n"
" CV 162,182,181\n"
" MA 184,152,164\n"
" v_cndmask_b32 v168,v162,v216,vcc\n"
" v_cndmask_b32 v169,v162,v216,vcc\n"
" MZ 192,196,160\n"
" v_mov_b32_dpp v170,v168 quad_perm:[2,3,0,1] row_mask:0xf bank_mask:0xf\n"
" v_mov_b32_dpp v171,v168 quad_perm:[2,3,0,1] row_mask:0xf bank_mask:0xf\n"
" MA 188,156,164\n"
" RD 208,249,15360\n"
" RD 212,249,16384\n"
" MA 184,200,168\n"
" MA 184,204,168\n"
" v_add_f32_dpp v192,v192,v192 quad_perm:[2,3,0,1] row_mask:0xf bank_mask:0xf\n"
" s_waitcnt lgkmcnt(1)\n"
" MA 188,208,168\n"
" v_rcp_f32 v192,v192\n"
" s_waitcnt lgkmcnt(0)\n"
" MA 188,212,168\n"
" s_nop 3\n"
" v_mul_f32 v184,v184,v192\n"
" v_mul_f32 v185,v185,v192\n"
" v_mul_f32 v186,v186,v192\n"
" v_mul_f32 v187,v187,v192\n"
" v_mul_f32 v188,v188,v192\n"
" v_mul_f32 v189,v189,v192\n"
" v_mul_f32 v190,v190,v192\n"
" v_mul_f32 v191,v191,v192\n"
" CV 172,184,188\n"
" CV 173,185,189\n"
" CV 174,186,190\n"
" CV 175,187,191\n"
MT(48)
" s_nop 1\n"
" SV 216,172,64\n"
" SV 220,172,72\n"
" SV 224,172,80\n"
" SV 228,172,88\n"
" AD2 216\n"
" CV 128,216,217\n"
" SV 232,172,96\n"
" AD2 220\n"
" CV 129,220,221\n"
" SV 236,172,104\n"
" AD2 224\n"
" CV 130,224,225\n"
" SV 240,172,112\n"
" AD2 228\n"
" CV 131,228,229\n"
" SV 244,172,120\n"
" buffer_store_dwordx4 v[128:131],v250,%3,s65 offen sc1\n"
" AD2 232\n"
" AD2 236\n"
" AD2 240\n"
" CV 132,232,233\n"
" AD2 244\n"
" CV 133,236,237\n"
" CV 134,240,241\n"
" CV 135,244,245\n"
" buffer_store_dwordx4 v[132:135],v250,%3,s84 offen sc1\n"
MT(50)
" RD 160,249,17408\n"
" ZR 168,16\n"
" ZR 216,16\n"
" s_add_u32 s68,s68,16384\n"
" s_cmp_lg_u32 s64,0\n"
" s_cbranch_scc1 RL_other\n"
" s_add_u32 s82,s65,s69\n"
" s_add_u32 s83,s82,4096\n"
" s_lshr_b32 s84,s69,7\n"
" s_add_u32 s84,s84,s68\n"
" s_mov_b32 s80,0\n"
" s_waitcnt lgkmcnt(0)\n"
" s_sleep " STR(DL1) "\n"
MT(52)
"RL_red:\n"
" buffer_load_dwordx4 v[128:131],v249,%3,s82 offen sc1\n"
" buffer_load_dwordx4 v[132:135],v249,%3,s83 offen sc1\n"
" buffer_load_dwordx4 v[136:139],v249,%3,s82 offen offset:1024 sc1\n"
" buffer_load_dwordx4 v[140:143],v249,%3,s83 offen offset:1024 sc1\n"
" buffer_load_dwordx4 v[144:147],v249,%3,s82 offen offset:2048 sc1\n"
" buffer_load_dwordx4 v[148:151],v249,%3,s83 offen offset:2048 sc1\n"
" buffer_load_dwordx4 v[152:155],v249,%3,s82 offen offset:3072 sc1\n"
" buffer_load_dwordx4 v[156:159],v249,%3,s83 offen offset:3072 sc1\n"
" s_waitcnt vmcnt(7)\n"
" MZ 164,160,128\n"
" s_waitcnt vmcnt(6)\n"
" MA 164,160,132\n"
" s_waitcnt vmcnt(5)\n"
" MA 164,160,136\n"
" s_waitcnt vmcnt(4)\n"
" MA 164,160,140\n"
" s_waitcnt vmcnt(3)\n"
" MA 164,160,144\n"
" s_waitcnt vmcnt(2)\n"
" MA 164,160,148\n"
" s_waitcnt vmcnt(1)\n"
" MA 164,160,152\n"
" s_waitcnt vmcnt(0)\n"
" MA 164,160,156\n"
" s_nop 7\n"
" v_cmp_u_f32 vcc,v164,v164\n"
" v_add_f32_dpp v164,v164,v164 row_ror:8 row_mask:0xf bank_mask:0xf\n"
" v_add_f32_dpp v165,v165,v165 row_ror:8 row_mask:0xf bank_mask:0xf\n"
" v_add_f32_dpp v166,v166,v166 row_ror:8 row_mask:0xf bank_mask:0xf\n"
" v_add_f32_dpp v167,v167,v167 row_ror:8 row_mask:0xf bank_mask:0xf\n"
" s_cbranch_vccnz RL_failr\n"
"RL_okr:\n"
" s_mov_b64 exec,0x00FF00FF\n"
" CV 164,164,165\n"
" CV 165,166,167\n"
" buffer_store_dwordx2 v[164:165],v255,%3,s84 offen sc1\n"
" s_mov_b64 exec,-1\n"
"RL_join:\n"
MT(54)
" s_barrier\n"
" s_add_u32 s78,s78,1\n"
" s_mov_b32 s81,s67\n"
" s_mov_b32 s67,s65\n"
" s_mov_b32 s65,s66\n"
" s_mov_b32 s66,s81\n"
" v_xor_b32 v253,16384,v253\n"
MTACC
" s_sleep " STR(DL2) "\n"
" s_branch RL_loop\n"
"RL_other:\n"
" v_mov_b64 v[164:165],-1\n"
" v_mov_b64 v[166:167],-1\n"
" s_add_u32 s82,s67,s71\n"
" s_mov_b32 m0,s76\n"
" s_add_u32 s86,s78,2\n"
" s_min_u32 s86,s86,62\n"
" s_mov_b32 s80,0\n"
" s_cmp_lt_u32 s85,s86\n"
" s_cbranch_scc1 RL_failf\n"
"RL_okf:\n"
" s_xor_b32 s72,s72,128\n"
" s_sleep " STR(DL3) "\n"
" .if (" STR(OPT) "&1)==0\n"
" buffer_load_dwordx4 v249,%3,s70 offen sc1 lds\n"
" buffer_load_dwordx4 v249,%3,s70 offen offset:1024 sc1 lds\n"
" buffer_load_dwordx4 v249,%3,s70 offen offset:2048 sc1 lds\n"
" buffer_load_dwordx4 v249,%3,s70 offen offset:3072 sc1 lds\n"
" .endif\n"
" buffer_load_dword v203,off,%3,s72 sc1\n"
" s_sleep " STR(DL4) "\n"
" .if (" STR(OPT) "&2)==0\n"
" EXPORT\n"
" .endif\n"
" .if (" STR(OPT) "&4)==0\n"
" buffer_store_dwordx4 v[164:167],v249,%3,s82 offen sc1\n"
" buffer_store_dwordx4 v[164:167],v249,%3,s82 offen offset:1024 sc1\n"
" buffer_store_dwordx4 v[164:167],v249,%3,s82 offen offset:2048 sc1\n"
" .endif\n"
" s_add_u32 s70,s70,0x80000\n"
" s_add_u32 s74,s74,s75\n"
" s_xor_b32 s76,s76,16384\n"
" s_branch RL_join\n"
"RL_failm:\n"
" SPIN RL_okm,1\n"
" buffer_load_dwordx4 v[128:131],v254,%3,s68 offen sc1\n"
" buffer_load_dwordx4 v[132:135],v254,%3,s68 offen offset:1024 sc1\n"
" ZR 168,4\n"
" s_waitcnt vmcnt(0)\n"
" s_branch RL_retry\n"
"RL_failr:\n"
" .set zi,129\n"
" .rept 15\n"
" v_max3_u32 v128,v128,v[zi],v[zi+1]\n"
" .set zi,zi+2\n"
" .endr\n"
" v_max_u32 v128,v128,v159\n"
" v_cmp_eq_u32 vcc,-1,v128\n"
" s_cbranch_vccz RL_okr\n"
" SPIN RL_okr,1024\n"
" s_branch RL_red\n"
"RL_failf:\n"
" SPIN RL_okf,0x100000\n"
" buffer_load_dword v200,off,%3,s72 sc1\n"
" s_waitcnt vmcnt(0)\n"
" v_readfirstlane_b32 s85,v200\n"
" s_nop 3\n"
" s_cmp_lt_u32 s85,s86\n"
" s_cbranch_scc1 RL_failf\n"
" s_branch RL_okf\n"
"RL_last:\n"
" s_cmp_eq_u32 s64,0\n"
" s_cbranch_scc1 2f\n"
" s_waitcnt lgkmcnt(0)\n"
" EXPORT\n"
"2:\n"
" v_mov_b32 v202,64\n"
" s_waitcnt vmcnt(0) lgkmcnt(0)\n"
" s_barrier\n"
" buffer_store_dword v202,off,%3,s73 sc1\n"
" s_nop 7\n"
  :"+{s[64:79]}"(sc),"+{v[248:255]}"(vc),"+{s[88:95]}"(pc)
  :"s"(rw),"{a[0:31]}"(wa[0]),"{a[32:63]}"(wa[1]),"{a[64:95]}"(wa[2]),"{a[96:127]}"(wa[3]),"{a[128:159]}"(wa[4]),"{a[160:191]}"(wa[5]),"{a[192:223]}"(wa[6]),"{a[224:255]}"(wa[7]),
   "{v[0:31]}"(wv[0]),"{v[32:63]}"(wv[1]),"{v[64:95]}"(wo[0]),"{v[96:127]}"(wo[1])
  :"memory","scc","vcc",CLOB);
  dead=sc[13];
  TS(3) TC(9,sc[15]&1023) TC(10,(sc[15]>>10)&1023)
}
#define DY 3
#define DW 5
#define GL __attribute__((address_space(1)))
#define XB 0
#define DH 8
#define DZ 24
DI u32 mx4(u32 m,u32x4 v){ return max(max(max(m,v.x),v.y),max(v.z,v.w)); }
DI u32x4 pk8(f32x4 a,f32x4 b){ return u32x4{pk(a.x,b.x),pk(a.y,b.y),pk(a.z,b.z),pk(a.w,b.w)}; }
DI u32x4 pkc(f32x4 a,f32x4 b){ return u32x4{pk(a.x,a.y),pk(a.z,a.w),pk(b.x,b.y),pk(b.z,b.w)}; }
DI void roleB1(const Args& A,char* ws,int h,int par,const int w,int& dead){
  const int l=lane(),g=l>>4,n=l&15;
  rsrc_t rw=mk(ws,WS_SZ); const u32 l16=l*16;
  enum{LA=3,L_B=65536,L_M=139264};
  UNR for(int i=0;i<8;i++) dma16(rw,l16,O_XF+par*32768+(w*8+i)*1024,(w*8+i)*1024);
  u32x4 wq[16][2],wk[16][2],wv[16][2];
  { rsrc_t rk=mk(A.wk,4*MB),rv=mk(A.wv,4*MB),rq=mk(A.wq,4*MB); const u32 base=32768+w*28672;
    const u32 va=g*8192+(n>>2)*65536+(n&3)*16,vb=(l>>5)*8192+((l>>3)&3)*65536+(l&7)*16; u32 ra=base+l*4,rb=base+l*8+(w&1)*4; PIN(ra); PIN(rb);
    stream<32,7>(base,[&](int c,u32 d){ u32 so=h*256; asm volatile("":"+s"(so),"+s"(d));
        UNR for(int i=0;i<4;i++){ if(c>=8&&c<24) dmaw(rv,vb,so+(w>>1)*128+((c-8)*32+2*i)*8192,d+i*1024);
          else dmaw(c<8?rk:rq,va,so+w*64+((c&7)*64+(i>>1)*32+(i&1)*4)*8192,d+i*1024); } },
      [&](int c,u32 o){ u32 t[16]; u32x4 f0,f1;
        if(c>=8&&c<24){ UNR for(int i=0;i<8;i++) t[i]=LDS(u32,rb+o+i*512); mkfr(t,f0,f1);
          if(c<17){ f0=toA(f0); f1=toA(f1); } else { PIN(f0); PIN(f1); } wv[c-8][0]=f0; wv[c-8][1]=f1; }
        else { UNR for(int i=0;i<16;i++) t[i]=LDS(u32,ra+o+i*256);
          UNR for(int q=0;q<2;q++){ mkfr(t+8*q,f0,f1); if(c<8){ wk[2*c+q][0]=toA(f0); wk[2*c+q][1]=toA(f1); } else { PIN(f0); PIN(f1); wq[2*(c&7)+q][0]=f0; wq[2*(c&7)+q][1]=f1; } } } }); }
  int bi=0,cf=0,cg=0; const u32 fo=O_FL+F_O+((par*2+(w&1))*32+h)*128+(w>>1)*4; vm0(); TS(1)
  for(int i=0;i<32+LA;i++){
    const int s=2*i+par,sa=s-2*LA; const u32 lb=L_B+bi*24576+l16; u32x4 pq[2],pk_[2],pv[2];
    bool pub=i>LA; u32 fl=pub?LDS(u32,151552+w*1024+l16):0; bar();
    if(i>=LA){
      if(__any(fl<(u32)sa+1)){ if(pub){ vm0(); st1c(rw,0,fo,sa-1); pub=false; } waitf(rw,O_FL+F_KM+2*h*128,2,sa+1,dead,128); CNT(cf,1) }
      if(i==32) TS(2) if(i==34) TS(3)
    }
#define KMD(j) { if(j==0) dma16(rw,(2*(n>>2)+(n&1))*256+g*16,O_KM+(sa*32+h)*2048+w*64,L_M+w*1024); \
      else dma16(rw,l16,O_VM+(sa*32+h)*8192+(2*w+j-1)*1024,L_M+4096+(2*w+j-1)*1024); }
    if(i<32){
      f32x4 cq[2][2]={},ck[2][2]={},cv[2][2]={}; u32 xs=O_XF+(s+2)*32768+w*8192,xd=((i+1)&1)*32768+w*8192;
      u32x4 xx[2][2]; const u32 xb=(i&1)*32768+l16; xx[0][0]=LDS(u32x4,xb); xx[0][1]=LDS(u32x4,xb+16384);
      UNR for(int ks=0;ks<16;ks++){
        UNR for(int kt=0;kt<2;kt++){ u32x4 x=xx[ks&1][kt];
          ck[kt][0]=mfma(wk[ks][0],x,ck[kt][0]); SB(); xx[(ks+1)&1][kt]=LDS(u32x4,xb+(((ks+1)&15)+16*kt)*1024); SB();
          cv[kt][0]=mfma(x,wv[ks][0],cv[kt][0]); ck[kt][1]=mfma(wk[ks][1],x,ck[kt][1]); cv[kt][1]=mfma(x,wv[ks][1],cv[kt][1]); SB(); }
        if(ks<3) KMD(ks) else if(ks<11){ dma16(rw,l16,xs,xd); xs+=1024; xd+=1024; asm volatile("":"+s"(xs),"+s"(xd)); }
        SB(); }
      const u32 gb=O_KV+(s*32+h)*16384;
      UNR for(int ks=0;ks<16;ks++){ const int j=ks>>3,q=ks&3;
        UNR for(int kt=0;kt<2;kt++){ u32x4 x=xx[ks&1][kt];
          cq[kt][0]=mfma(wq[ks][0],x,cq[kt][0]); SB(); if(ks<15) xx[(ks+1)&1][kt]=LDS(u32x4,xb+(ks+1+16*kt)*1024); SB();
          cq[kt][1]=mfma(wq[ks][1],x,cq[kt][1]); SB();
          if(kt==0){ if(ks&4) pv[j][q]=pk(cv[q>>1][j][(q&1)*2],cv[q>>1][j][(q&1)*2+1]); else pk_[j][q]=pk(ck[j][0][q],ck[j][1][q]); SB(); } }
        if(ks==3){ vm0(); if(pub) st1c(rw,0,fo,sa-1); pub=false; }
        if(q==3){ if(ks&4) st4c(rw,l16,gb+8192+(2*w+j)*1024,pv[j]); else st4c(rw,l16,gb+(j*4+w)*1024,pk_[j]); }
        SB(); }
      UNR for(int j=0;j<2;j++) pq[j]=toA(pk8(cq[j][0],cq[j][1]));

    } else { KMD(0) KMD(1) KMD(2) }
    if(i>=LA){
      const int qt=w&1,eh=w>>1; f32x4 s0={},s1={},sm={}; u32x4 qf[4],kf[4];
      UNR for(int k=0;k<4;k++){ qf[k]=LDS(u32x4,lb+16384+(qt*4+k)*1024); kf[k]=LDS(u32x4,lb+k*1024); } SB();
      UNR for(int k=0;k<4;k++){ s0=mfma(kf[k],qf[k],s0); SB(); kf[k]=LDS(u32x4,lb+(4+k)*1024); SB(); }
      UNR for(int k=0;k<4;k++) s1=mfma(kf[k],qf[k],s1);
      SB(); if(i>=32) vm0();
      if(pub) st1c(rw,0,fo,sa-1);
      dma16(rw,(l16&16)*8,O_FL+F_KM+2*h*128,151552+w*1024); bar();
      UNR for(int k=0;k<4;k++) kf[k]=LDS(u32x4,L_M+k*1024+l16); SB();
      UNR for(int k=0;k<4;k++){ sm=mfma(kf[k],qf[k],sm); SB(); kf[k]=LDS(u32x4,lb+8192+(eh*4+k)*1024); qf[k]=LDS(u32x4,L_M+4096+(eh*4+k)*1024+l16); SB(); }
      const int dq=8*(n>>2)+4*qt+(n&3)-8*g; float pe[10],mx=fmaxf(sm.x,sm.y)*CEXP;
      UNR for(int j=0;j<8;j++){ pe[j]=j<=dq?(j<4?s0[j&3]:s1[j&3])*CEXP:-1.4426950e9f; mx=fmaxf(mx,pe[j]); }
      pe[8]=sm.x*CEXP; pe[9]=sm.y*CEXP; mx=gmax(mx); float su=0;
      UNR for(int j=0;j<10;j++){ pe[j]=__builtin_amdgcn_exp2f(pe[j]-mx); su+=pe[j]; }
      su=__builtin_amdgcn_rcpf(gsum(su));
      u32x4 pf={pk(pe[0],pe[1]),pk(pe[2],pe[3]),pk(pe[4],pe[5]),pk(pe[6],pe[7])}; u32 p1=pk(pe[8],pe[9]); u32x4 pm={p1,p1,p1,p1};
      f32x4 d[4];
      UNR for(int t=0;t<4;t++){ d[t]=mfma(kf[t],pf,f32x4{0,0,0,0});
        d[t]=mfma(u32x4{qf[(t&1)*2][t&2],qf[(t&1)*2][(t&2)+1],qf[(t&1)*2+1][t&2],qf[(t&1)*2+1][(t&2)+1]},pm,d[t])*su; }
      SB(); vm0();
      UNR for(int j=0;j<2;j++) st4c(rw,l16,O_OF+((2*sa+qt)*128+h*4+eh*2+j)*1024,
        u32x4{pk(d[0][2*j],d[1][2*j]),pk(d[2][2*j],d[3][2*j]),pk(d[0][2*j+1],d[1][2*j+1]),pk(d[2][2*j+1],d[3][2*j+1])});
    } else vm0();
    if(i<32){ bar();
      if(w==0) st1c(rw,0,O_FL+F_KV+(2*h+par)*128,s+1);
      UNR for(int j=0;j<2;j++){ LDS(u32x4,lb+(j*4+w)*1024)=pk_[j]; LDS(u32x4,lb+8192+(2*w+j)*1024)=pv[j]; LDS(u32x4,lb+16384+(j*4+w)*1024)=pq[j]; } }
    bi=bi==LA-1?0:bi+1;
  }
  vm0(); st1c(rw,0,fo,64); bar();
  TC(9,cf) TC(10,cg)
}
DI float gelu(float x){ float u=x*(1.f+0.044715f*x*x)*-2.3022082f; return x*__builtin_amdgcn_rcpf(1.f+__builtin_amdgcn_exp2f(u)); }
DI void roleB2(const Args& A,char* ws,int grp,int j,const int w,int& dead){
  const int l=lane(),g=l>>4,n=l&15;
  rsrc_t rw=mk(ws,WS_SZ); const u32 l16=l*16;
  waitf(rw,O_FL+F_KV+(grp*16+j)*128,1,1,dead);
  u32x4 wo[32][2],wi[16][2];
  { rsrc_t ro=mk(A.wo,4*MB); u32 t[2][24];
    UNR for(int b=0;b<=11;b++){
      if(b<11){ u32 so=j*64+w*MB; asm volatile("":"+s"(so)); UNR for(int i=0;i<24;i++){ int ks=b*3+i/8; if(ks<32) t[b&1][i]=ld1(ro,g*16384+n*4+(i&3)*1024,so+((ks>>1)*64+(ks&1)*8+(i&4))*1024); } }
      SB();
      if(b>0){ UNR for(int q=0;q<3;q++){ int ks=(b-1)*3+q; if(ks<32){ u32x4 f0,f1; mkfr(&t[(b-1)&1][q*8],f0,f1);
        if(ks<30){ wo[ks][0]=toA(f0); wo[ks][1]=toA(f1);} else { wo[ks][0]=f0; wo[ks][1]=f1; } } } }
      SB(); } }
  const int un=j+16*w,isz2=un>=22,u=un-22*isz2;
  ldw<16,0>(wi,mk(A.win,512*1376*2),(un<44&&u*32+2*n<688)?g*8*2752+n*4:0x7FFFF000u,(un<44?isz2*1376+u*64:0),2752);
  { const u32x4 sv={SENT,SENT,SENT,SENT}; const int c=(grp*16+j)*4+w;
    UNR for(int i=0;i<8;i++) st4c(rw,l16+(i&3)*1024,O_HF+(c*8+(i&4))*1024,sv);
    UNR for(int i=0;i<11;i++){ st4c(rw,l16+(i&3)*1024,O_Z1+(c*11+(i&12))*1024,sv); st4c(rw,l16+(i&3)*1024,O_Z2+(c*11+(i&12))*1024,sv); st4c(rw,l16+(i&3)*1024,O_UF+(c*11+(i&12))*1024,sv); }
    UNR for(int i=0;i<2;i++) st4c(rw,l16,O_ST+(c*2+i)*1024,sv);
    vm0(); bar(); if(w==0) st1c(rw,0,O_FL+F_FI+c*32,1); }
  waitf(rw,O_FL+F_FI,64,1,dead,128);
  TS(1)
  enum{L_X=131072,L_P=147456-2048,L_S=153600};
  const u32x4 one={0x3F803F80u,0x3F803F80u,0x3F803F80u,0x3F803F80u}; f32x4 cs0={},cs1={};
  if(!isz2){ UNR for(int k=0;k<16;k++){ cs0=mfma(wi[k][0],one,cs0); cs1=mfma(wi[k][1],one,cs1); } }
  else     { UNR for(int k=0;k<16;k++){ cs0=mfma(one,wi[k][0],cs0); cs1=mfma(one,wi[k][1],cs1); } }
  u32 lx=L_X+l16,lo=w*32768+l16; PIN(lx); PIN(lo); PIN(cs0); PIN(cs1);
  int cf=0,cg=0; rsrc_t rx=mk(A.x,2*MB); const u32 fv=(l&7)*128+(l&8)/2;
  PT0
  for(int it=0;;it++){
    const int blk=grp+4*it,pb=blk-4; const u32 fo=O_FL+F_O+(grp*32+w*8)*128,need=(blk>>1)+1,ob=O_OF+(blk*128+w*32)*1024;
    bool rdy=false;
    if(it){ const int s=pb>>1,kt=pb&1; int sp=0;
      if(w) __builtin_amdgcn_s_sleep(DH+5); else __builtin_amdgcn_s_sleep(DH);
      PT(0) u32x4 x[16]; u32 fl;
      for(;;){ asm volatile("":::"memory"); UNR for(int i=0;i<4;i++) x[i]=ld4c(rw,l16+i*1024,O_HF+(pb*16+w*4)*1024);
        fl=ld1c(rw,fv,fo); u32 mx=0; UNR for(int i=0;i<4;i++) mx=mx4(mx,x[i]);
        if(dead||!__any(mx==SENT)) break; if(++sp>SPINCAP){dead=1;break;} }
      PT(1) UNR for(int i=0;i<4;i++) LDS(u32x4,lx+(w*4+i)*1024)=x[i];
      rdy=!__any(fl<need); bar();
      UNR for(int k=0;k<16;k++) x[k]=LDS(u32x4,lx+k*1024); SB();
      if(it==32) TS(3)
      f32x4 z0={},z1={};
#define OD if((k&3)==0){ SB(); if(rdy) dmaN<1>(rw,l16,ob+k*1024,w*32768+k*1024); SB(); }
      if(w==3){ UNR for(int k=0;k<16;k++){ z0=mfma(x[k],x[k],z0); z1=mfma(one,x[k],z1); OD }
        float mu=z1.x*(1.f/512),q=(n&2)?((n&1)?z0.w:z0.z):((n&1)?z0.y:z0.x);
        if(g==n>>2){ LDS(float,L_S+n*4)=mu; LDS(float,L_S+64+n*4)=rsqrtf(fmaxf(q*(1.f/512)-mu*mu,0.f)+1e-5f); } }
      else if(!isz2){ UNR for(int k=0;k<16;k++){ z0=mfma(wi[k][0],x[k],z0); z1=mfma(wi[k][1],x[k],z1); OD } }
      else     { UNR for(int k=0;k<16;k++){ z0=mfma(x[k],wi[k][0],z0); z1=mfma(x[k],wi[k][1],z1); OD } }
      bar();
      if(un<44){ f32x4 mu,rs;
        if(isz2){ mu=LDS(f32x4,L_S+g*16); rs=LDS(f32x4,L_S+64+g*16); } else { float a=LDS(float,L_S+n*4),b=LDS(float,L_S+64+n*4); mu=f32x4{a,a,a,a}; rs=f32x4{b,b,b,b}; }
        UNR for(int i=0;i<4;i++){ z0[i]=(z0[i]-mu[i]*cs0[i])*rs[i]; z1[i]=(z1[i]-mu[i]*cs1[i])*rs[i]; }
        UNR for(int i=0;i<4;i++){ z0[i]=gelu(z0[i]); z1[i]=gelu(z1[i]); }
        if(!isz2) st4c(rw,l16,O_Z1+(pb*22+u)*1024,pk8(z0,z1));
        else { u32x4 v=pkc(z0,z1); st4c(rw,l16,O_Z2+(s*44+2*u+kt)*1024,v);
          f32x4 a,b; UNR for(int i=0;i<4;i++){ float p=(i&1)?bhi(v[i>>1]):blo(v[i>>1]),q=(i&1)?bhi(v[2+(i>>1)]):blo(v[2+(i>>1)]); a[i]=rsum16(p+q); b[i]=rsum16(p*p+q*q); }
          if(n<2) st4c(rw,g*32+n*16,O_ST+(s*22+u)*256+kt*128,__builtin_bit_cast(u32x4,n?f32x4{a.z,a.w,b.z,b.w}:f32x4{a.x,a.y,b.x,b.y})); } }
      if(it==32){ TC(9,cf) TC(10,cg) PTE break; }
      PT(2)
    }
    if(w==0) dmaw(rx,(8*(n>>2)+(n&3))*1024+g*16,((blk>>1)*32+4*(blk&1))*1024+j*64,154624);
    if(!rdy){ waitf(rw,fo,64,need,dead,0,fv); CNT(cf,1) if(it==31) TS(2) dmaN<4>(rw,l16,ob,w*32768); }
    PT(3) f32x4 c0={},c1={}; u32x4 o[2][4];
    UNR for(int b=0;b<=8;b++){
      if(b<8) asm volatile("s_waitcnt vmcnt(%0)"::"n"(b<4?12:28-4*b):"memory");
      UNR for(int i=0;i<4;i++){ if(b<8) o[b&1][i]=LDS(u32x4,lo+(b*4+i)*1024); SB();
        if(b>0){ c0=mfma(wo[(b-1)*4+i][0],o[(b-1)&1][i],c0); c1=mfma(wo[(b-1)*4+i][1],o[(b-1)&1][i],c1); } SB(); }
      if(b<4) dmaN<1>(rw,l16,ob+16384+b*4096,w*32768+16384+b*4096);
      SB(); }
    PT(4)
    if(w){ LDS(f32x4,L_P+w*2048+l16)=c0; LDS(f32x4,L_P+w*2048+1024+l16)=c1; }
    bar();
    if(w==0){ UNR for(int q=1;q<4;q++){ c0+=LDS(f32x4,L_P+q*2048+l16); c1+=LDS(f32x4,L_P+q*2048+1024+l16); }
      u32x4 a=pk8(c0,c1),hh,xr=LDS(u32x4,154624+l16); UNR for(int i=0;i<4;i++) hh[i]=pk(blo(a[i])+blo(xr[i]),bhi(a[i])+bhi(xr[i]));
      st4c(rw,l16,O_HF+(blk*16+j)*1024,hh); }
    PT(5)
  }
}
DI void roleB3(const Args& A,char* ws,int tb,const int w,int& dead){
  const int l=lane(),g=l>>4,n=l&15;
  rsrc_t rw=mk(ws,WS_SZ),rs_=mk(A.wsp,8*MB); const u32 l16=l*16;
  f32x4 c[12][2]={}; float cr[2]={0,0}; u32x4 z[12],y[12]={}; int cf=0;
  const int tk=tb*32+8*(n>>2)+(n&3); const int nt=w<2?12:10,t0=w<2?12*w:10*w+4;
  UNR for(int i=0;i<DW;i++) __builtin_amdgcn_s_sleep(37);
  for(int i=0;i<32;i++) dma16(rs_,tk*4096+g*16,(w*16+(i>>2)*2+(i&1))*64+(i&2)*8192,((w*16+(i>>2)*2+(i&1))*2+((i>>1)&1))*1024);
  vm0(); TS(1) waitf(rw,O_FL+F_O+tb*128,1,7,dead); waitf(rw,O_FL+F_FI,64,1,dead,128); bar();
  float bsp[2]; UNR for(int kt=0;kt<2;kt++){ bsp[kt]=blo(((const GL unsigned short*)A.bsp)[tk+4*kt]); PIN(bsp[kt]); }
  const u32 v0=n*256+g*32,v1=n<6?v0+4096:0x7FFFF000u,sb=O_ST+(w>>1)*128+(w&1)*16,lw=l16+w*4; u32x4 ta={SENT},tc={};
  for(int s=0;s<64;s++){
    int sp=0; const bool ly=s==max(tb,61);
    while(!dead&&__any(mx4(mx4(0,ta),tc)==SENT)){ if(++sp>SPINCAP){dead=1;break;} if(s<61) __builtin_amdgcn_s_sleep(DZ); else if(sp>1) __builtin_amdgcn_s_sleep(DY); ta=ld4c(rw,v0,sb+s*5632); tc=ld4c(rw,v1,sb+s*5632); }
    f32x4 st=__builtin_bit_cast(f32x4,ta)+__builtin_bit_cast(f32x4,tc); u32 d0=LDS(u32,s*2048+lw),d1=LDS(u32,s*2048+1024+lw); if(s==63) TS(2)
#define LDZ { UNR for(int i=0;i<12;i++) if(i<nt) z[i]=ld4c(rw,l16+(i&3)*1024,O_Z2+(s*44+t0+(i&12))*1024); \
      if(ly){ UNR for(int i=0;i<12;i++) if(i<nt) y[i]=ld4c(rw,l16,O_Z1+((tb*2+(i&1))*22+(t0>>1)+(i>>1))*1024); } }
    SB(); LDZ ta=ld4c(rw,v0,sb+s*5632+5632); tc=ld4c(rw,v1,sb+s*5632+5632); SB();
    UNR for(int i=0;i<4;i++) st[i]=rsum16(st[i])*(1.f/688);
    float ra=rsqrtf(fmaxf(st.z-st.x*st.x,0.f)+1e-5f),rb=rsqrtf(fmaxf(st.w-st.y*st.y,0.f)+1e-5f);
    d0=pk(blo(d0)*ra,bhi(d0)*rb); d1=pk(blo(d1)*ra,bhi(d1)*rb); LDS(u32,s*2048+lw)=d0; LDS(u32,s*2048+1024+lw)=d1;
    cr[0]+=blo(d0)*st.x+bhi(d0)*st.y; cr[1]+=blo(d1)*st.x+bhi(d1)*st.y;
    if(s==63){ UNR for(int kt=0;kt<2;kt++){ float v=gsum(cr[kt]); if(g==0) LDS(float,131072+kt*256+lw)=v; } }
    bar(); const u32x4 b0=LDS(u32x4,s*2048+l16),b1=LDS(u32x4,s*2048+1024+l16); SB();
    for(;;){ u32 mx=0; UNR for(int i=0;i<12;i++) if(i<nt) mx=mx4(mx,z[i]);
      if(ly){ UNR for(int i=0;i<12;i++) if(i<nt) mx=mx4(mx,y[i]); }
      if(dead||!__any(mx==SENT)) break; if(++sp>SPINCAP){dead=1;break;} CNT(cf,1) asm volatile("":::"memory"); LDZ }
    UNR for(int i=0;i<12;i++) if(i<nt){ const int o=(i&1)*2; u32x4 a={z[i&14][o],z[i&14][o+1],z[i|1][o],z[i|1][o+1]}; c[i][0]=mfma(a,b0,c[i][0]); c[i][1]=mfma(a,b1,c[i][1]); }
  }
  TS(3) TC(9,cf)
  UNR for(int kt=0;kt<2;kt++){ f32x4 cc=LDS(f32x4,131072+kt*256+n*16); float bs=bsp[kt]-(cc.x+cc.y+cc.z+cc.w);
    UNR for(int i=0;i<6;i++) if(2*i<nt){ u32x4 v; UNR for(int q=0;q<4;q++) v[q]=pk(blo(y[2*i+kt][q])*(c[2*i][kt][q]+bs),bhi(y[2*i+kt][q])*(c[2*i+1][kt][q]+bs));
      st4c(rw,l16,O_UF+((tb*2+kt)*22+(t0>>1)+i)*1024,v); } }
}
DI void roleF(const Args& A,char* ws,int tb,int nh,const int w,int& dead){
  const int l=lane(),g=l>>4,n=l&15;
  rsrc_t rw=mk(ws,WS_SZ),ro=mk(A.wout,688*1024); const u32 l16=l*16;
  u32x4 wf[22][4];
  { u32x2 t[2][16];
    UNR for(int b=0;b<=11;b++){
      if(b<11){ UNR for(int i=0;i<16;i++) t[b&1][i]=ld2(ro,g*8192+n*8+(b*64+(i>>3)*32+(i&7))*1024,nh*512+w*128); }
      SB();
      if(b>0){ UNR for(int q=0;q<8;q++){ const u32x2* p=&t[(b-1)&1][(q>>2)*8]; int hi=(q>>1)&1; u32x4 f;
          UNR for(int i=0;i<4;i++) f[i]=(q&1)?phi(p[2*i][hi],p[2*i+1][hi]):plo(p[2*i][hi],p[2*i+1][hi]);
          if(b>7) PIN(f); wf[(b-1)*2+(q>>2)][q&3]=b<=7?toA(f):f; } }
      SB(); } }
  waitf(rw,O_FL+F_FI,64,1,dead,128); TS(5)
  const u32 ub=O_UF+(tb*44+w*11)*1024; int sp=0;
  while(!dead&&__any(ld1c(rw,min(n,10)*1024,ub)==SENT)){ if(++sp>SPINCAP){dead=1;break;} asm volatile("s_sleep 2":::"memory"); }
  TS(6)
  u32x4 hf[2][2],uf[11];
  for(;;){ asm volatile("":::"memory"); u32 mx=0;
    UNR for(int i=0;i<11;i++) uf[i]=ld4c(rw,l16+(i&3)*1024,ub+(i&12)*1024);
    UNR for(int i=0;i<4;i++) hf[i>>1][i&1]=ld4c(rw,g*512+(i&1)*256+n*16,O_HF+((tb*2+(i>>1))*16+8*nh+2*w)*1024);
    UNR for(int i=0;i<11;i++){ mx=mx4(mx,uf[i]); LDS(u32x4,(w*11+i)*1024+l16)=uf[i]; }
    UNR for(int i=0;i<4;i++) mx=mx4(mx,hf[i>>1][i&1]);
    if(dead||!__any(mx==SENT)) break; if(++sp>SPINCAP){dead=1;break;} }
  bar();
  f32x4 c[2][4]={};
  u32x4 x[2][4]; UNR for(int i=0;i<4;i++) x[0][i]=LDS(u32x4,((i&1)*22+(i>>1))*1024+l16);
  UNR for(int k=0;k<11;k++){ if(k<10){ UNR for(int i=0;i<4;i++) x[(k+1)&1][i]=LDS(u32x4,((i&1)*22+2*k+2+(i>>1))*1024+l16); } SB();
    UNR for(int i=0;i<4;i++) UNR for(int t=0;t<4;t++) c[i&1][t]=mfma(wf[2*k+(i>>1)][t],x[k&1][i],c[i&1][t]); SB(); }
  UNR for(int kt=0;kt<2;kt++) UNR for(int q=0;q<2;q++){ u32x4 v;
    UNR for(int i=0;i<4;i++){ u32 a=pk(c[kt][(i&1)*2][2*q+(i>>1)],c[kt][(i&1)*2+1][2*q+(i>>1)]); v[i]=pk(blo(a)+blo(hf[kt][q][i]),bhi(a)+bhi(hf[kt][q][i])); }
    ((GL u32x4*)A.out)[(tb*32+8*(n>>2)+4*kt+(n&3))*64+nh*32+w*8+g*2+q]=v; }
}
DI Args AR(){ typedef const __attribute__((address_space(4))) u64* AP; AP p=(AP)__builtin_amdgcn_kernarg_segment_ptr(); asm volatile("":"+s"(p));
  return Args{(void*)p[0],(void*)p[1],(void*)p[2],(void*)p[3],(void*)p[4],(void*)p[5],(void*)p[6],(void*)p[7],(void*)p[8],(void*)p[9],(void*)p[10],(void*)p[11]}; }
__global__ void __launch_bounds__(256,1) main_kernel(Args A){
  __shared__ __attribute__((aligned(1024))) char smem[LDS_SZ];
  asm volatile(""::"v"(smem):"memory");
  char* ws=wsbase(A.ws); const int b=blockIdx.x,x=b&7,y=b>>3; int dead=0; const int w=__builtin_amdgcn_readfirstlane(threadIdx.x>>6);
  TS(0)
  if(y<16){
    if(x<4) roleR(AR(),ws,8*x+(y>>1),y&1,w,dead); else roleB1(AR(),ws,8*(x-4)+(y>>1),y&1,w,dead);
    TS(4) roleF(AR(),ws,8*x+(y>>1),y&1,w,dead);
  }
  else if(x<4) roleB3(AR(),ws,x*16+y-16,w,dead);
  else roleB2(AR(),ws,x-4,y-16,w,dead);
  TS(7)
}
extern "C" void kernel_launch(void* const* d_in,const int* in_sizes,int n_in,void* d_out,int out_size,void* d_ws,size_t ws_size,hipStream_t stream){
  Args A{d_in[0],d_in[1],d_in[2],d_in[3],d_in[4],d_in[5],d_in[8],d_in[9],d_in[10],d_in[11],d_out,d_ws};
  pre_kernel<<<256,256,0,stream>>>(A);
  main_kernel<<<256,256,0,stream>>>(A);
}
```

```cpp
#define RG 63
#include <hip/hip_runtime.h>
#include <hip/hip_bf16.h>
typedef unsigned u32;
typedef unsigned long long u64;
typedef __attribute__((ext_vector_type(2))) u32 u32x2;
typedef __attribute__((ext_vector_type(4))) u32 u32x4;
typedef __attribute__((ext_vector_type(8))) u32 u32x8;
typedef __attribute__((ext_vector_type(2))) float f32x2;
typedef __attribute__((ext_vector_type(4))) float f32x4;
typedef __attribute__((ext_vector_type(2))) __bf16 bfx2;
typedef __attribute__((ext_vector_type(8))) __bf16 bfx8;
typedef __amdgpu_buffer_rsrc_t rsrc_t;
#define DI __device__ __forceinline__
#define UNR _Pragma("unroll")
#define SB() __builtin_amdgcn_sched_barrier(0)
#define SENT 0xFFFFFFFFu
#define SPINCAP 400000
#define MB (1u<<20)
#define O_M   (3*MB)
#define O_FL  (4*MB)
#define F_SEL 0
#define F_KV  1024
#define F_KM  (F_KV+8192)
#define F_O   (F_KM+8192)
#define F_U   (F_O+16384)
#define F_FI  (F_U+8192)
#define FL_SZ 65536
#define O_XF  (5*MB)
#define O_HF  (7*MB)
#define O_Z1  (9*MB)
#define O_Z2  (12*MB)
#define O_UF  (15*MB)
#define O_ST  (18*MB)
#define O_KM  (20*MB)
#define O_KV  (24*MB)
#define O_VM  (56*MB)
#define O_OF  (72*MB)
#define WS_SZ (88*MB)
#define LDS_SZ (152*1024)

DI rsrc_t mk(const void* p,u32 n){ return __builtin_amdgcn_make_buffer_rsrc((void*)p,0,n,0x00020000); }
DI u32x4 ld4(rsrc_t r,u32 v,u32 s){ return __builtin_amdgcn_raw_buffer_load_b128(r,v,s,0); }
DI u32x2 ld2(rsrc_t r,u32 v,u32 s){ return __builtin_amdgcn_raw_buffer_load_b64(r,v,s,0); }
DI u32   ld1(rsrc_t r,u32 v,u32 s){ return __builtin_amdgcn_raw_buffer_load_b32(r,v,s,0); }
DI u32x4 ld4c(rsrc_t r,u32 v,u32 s){ return __builtin_amdgcn_raw_buffer_load_b128(r,v,s,16); }
DI u32x2 ld2c(rsrc_t r,u32 v,u32 s){ return __builtin_amdgcn_raw_buffer_load_b64(r,v,s,16); }
DI u32   ld1c(rsrc_t r,u32 v,u32 s){ return __builtin_amdgcn_raw_buffer_load_b32(r,v,s,16); }
DI void st4c(rsrc_t r,u32 v,u32 s,u32x4 d){ __builtin_amdgcn_raw_buffer_store_b128(d,r,v,s,16); }
DI void st2c(rsrc_t r,u32 v,u32 s,u32x2 d){ __builtin_amdgcn_raw_buffer_store_b64(d,r,v,s,16); }
DI void st1c(rsrc_t r,u32 v,u32 s,u32 d){ __builtin_amdgcn_raw_buffer_store_b32(d,r,v,s,16); }
DI void dma16(rsrc_t r,u32 v,u32 s,u32 lds){ asm volatile("s_mov_b32 m0, %3\n s_nop 0\n buffer_load_dwordx4 %0, %1, %2 offen sc1 lds"::"v"(v),"s"(r),"s"(s),"s"(lds):"memory"); }
#define SCX "sc1"
template<int N4> DI void dmaN(rsrc_t r,u32 v,u32 s,u32 lds){ asm volatile("s_mov_b32 m0, %3\n .rept %4\n s_nop 0\n .irp o,0,1024,2048,3072\n buffer_load_dwordx4 %1, %2, %0 offen offset:\\o " SCX " lds\n .endr\n s_add_u32 m0,m0,4096\n s_add_u32 %0,%0,4096\n .endr":"+s"(s):"v"(v),"s"(r),"s"(lds),"n"(N4):"memory","scc"); }
DI void vm0(){ asm volatile("s_waitcnt vmcnt(0)":::"memory"); }
DI void bar(){ asm volatile("s_waitcnt lgkmcnt(0)\n s_barrier":::"memory"); }
DI u32 pk(float a,float b){ f32x2 v={a,b}; return __builtin_bit_cast(u32,__builtin_convertvector(v,bfx2)); }
DI float blo(u32 v){ return __uint_as_float(v<<16); }
DI float bhi(u32 v){ return __uint_as_float(v&0xFFFF0000u); }
DI u32 plo(u32 a,u32 b){ return __builtin_amdgcn_perm(b,a,0x05040100u); }
DI u32 phi(u32 a,u32 b){ return __builtin_amdgcn_perm(b,a,0x07060302u); }
DI f32x4 mfma(u32x4 a,u32x4 b,f32x4 c){ return __builtin_amdgcn_mfma_f32_16x16x32_bf16(__builtin_bit_cast(bfx8,a),__builtin_bit_cast(bfx8,b),c,0,0,0); }
DI float gmax(float x){ u32 u=__float_as_uint(x); auto a=__builtin_amdgcn_permlane16_swap(u,u,false,false); x=fmaxf(__uint_as_float(a[0]),__uint_as_float(a[1]));
  u=__float_as_uint(x); auto b=__builtin_amdgcn_permlane32_swap(u,u,false,false); return fmaxf(__uint_as_float(b[0]),__uint_as_float(b[1])); }
DI float gsum(float x){ u32 u=__float_as_uint(x); auto a=__builtin_amdgcn_permlane16_swap(u,u,false,false); x=__uint_as_float(a[0])+__uint_as_float(a[1]);
  u=__float_as_uint(x); auto b=__builtin_amdgcn_permlane32_swap(u,u,false,false); return __uint_as_float(b[0])+__uint_as_float(b[1]); }
template<int C> DI float dppf(float x){ return __int_as_float(__builtin_amdgcn_update_dpp(0,__float_as_int(x),C,0xF,0xF,true)); }
DI float rsum16(float x){ x+=dppf<0x128>(x); x+=dppf<0x124>(x); x+=dppf<0x122>(x); x+=dppf<0x121>(x); return x; }
template<class T> DI T toA(T v){ T o; asm("":"=a"(o):"0"(v)); return o; }
#define LDS(T,off) (*(__attribute__((address_space(3))) T*)(size_t)(off))
DI int lane(){ int l; asm volatile("v_mbcnt_lo_u32_b32 %0, -1, 0\n v_mbcnt_hi_u32_b32 %0, -1, %0":"=v"(l)); __builtin_assume(l>=0&&l<64); return l; }
DI void waitf(rsrc_t r,u32 off,u32 n,u32 need,int& dead,u32 str=4,u32 vo=0){
  if(n>1&&str) vo=(lane()&(n-1))*str; int sp=0;
  while(!dead){ u32 v=ld1c(r,vo,off); if(!__any(v<need)) break; if(++sp>SPINCAP){dead=1;break;} __builtin_amdgcn_s_sleep(2); }
  asm volatile("":::"memory");
}
DI void mkfr(const u32* t,u32x4& f0,u32x4& f1){
  f0=u32x4{plo(t[0],t[1]),plo(t[2],t[3]),plo(t[4],t[5]),plo(t[6],t[7])};
  f1=u32x4{phi(t[0],t[1]),phi(t[2],t[3]),phi(t[4],t[5]),phi(t[6],t[7])};
}
DI void dmaw(rsrc_t r,u32 v,u32 s,u32 lds){ asm volatile("s_mov_b32 m0, %3\n s_nop 0\n buffer_load_dwordx4 %0, %1, %2 offen lds"::"v"(v),"s"(r),"s"(s),"s"(lds):"memory"); }
template<int NC,int NR,class I,class U> DI void stream(u32 base,I issue,U use){
  UNR for(int c=0;c<NR;c++) issue(c,base+c*4096);
  UNR for(int c=0;c<NC;c++){ asm volatile("s_waitcnt vmcnt(%0)"::"n"(4*((NC-c<NR?NC-c:NR)-1)):"memory");
    use(c,(c%NR)*4096); SB(); asm volatile("s_waitcnt lgkmcnt(0)":::"memory");
    if(c+NR<NC) issue(c+NR,base+(c%NR)*4096); SB(); }
}
#define TC(i,v)
#define CNT(c,v)
#define PT(i)
#define PT0
#define PTE
#define TS(i) {}
#undef PT
#undef PT0
#undef PTE
#define PT(i)
#define PT0
#define PTE
struct Args{ const void *x,*mem0,*wq,*wk,*wv,*wo,*win,*wsp,*bsp,*wout; void *out,*ws; };
DI char* wsbase(void* ws){ return (char*)(((size_t)ws+(2*MB-1))&~(size_t)(2*MB-1)); }

__global__ void __launch_bounds__(256) pre_kernel(Args A){
  char* ws=wsbase(A.ws); rsrc_t rw=mk(ws,WS_SZ);
  int b=blockIdx.x,t=threadIdx.x,l=t&63,w=t>>6;
  u32x4 xv[2];
  UNR for(int i=0;i<2;i++) xv[i]=((const u32x4*)A.x)[(b*8+w*2+i)*64+l];
  if((b&127)==0){
    if(w==0){
      if(l<8) st1c(rw,l*2048,b*16384,SENT);
      vm0(); u32 mn[2]={~0u,~0u};
      UNR for(int i=0;i<8;i++){ int j=(i>>1)+(i&1)*4;
        u64 c0=__builtin_amdgcn_s_memtime(); asm volatile("s_waitcnt lgkmcnt(0)":::"memory");
        u32 v=ld1c(rw,0,b*16384+j*2048); asm volatile("s_waitcnt vmcnt(0)":"+v"(v)::"memory");
        u64 c1=__builtin_amdgcn_s_memtime(); mn[i&1]=min(mn[i&1],(u32)(c1-c0)); }
      if(l==0) *(u32*)(ws+O_FL+F_SEL+(b>>7)*4)=mn[1]<mn[0]?8192u:0u;
    }
    __syncthreads();
  }
  UNR for(int i=0;i<4;i++){ u32x4 v={SENT,SENT,SENT,SENT}; int c=i*256+t;
    if(b==192){ int cc=c&511; const u32x2* p=(const u32x2*)A.mem0+((cc>>2)&7)*128+(cc>>6)*16+(cc&3)*4+((cc>>5)&1); u32x2 a=p[0],e=p[2]; v=u32x4{a.x,a.y,e.x,e.y}; }
    ((u32x4*)ws)[b*1024+c]=v; }
  if(b>=4&&t<16) ((u32x4*)(ws+O_FL))[b*16+t]=u32x4{0,0,0,0};
  UNR for(int i=0;i<2;i++){
    float f[8],s=0; UNR for(int j=0;j<4;j++){ f[2*j]=blo(xv[i][j]); f[2*j+1]=bhi(xv[i][j]); s+=f[2*j]+f[2*j+1]; }
    float mu=gsum(rsum16(s))*(1.f/512),q=0; UNR for(int j=0;j<8;j++){ f[j]-=mu; q+=f[j]*f[j]; }
    float rs=rsqrtf(gsum(rsum16(q))*(1.f/512)+1e-5f);
    int tk=b*8+w*2+i,tau=tk&31,kt=(tau>>2)&1,c=4*(tau>>3)+(tau&3);
    ((u32x4*)(ws+O_XF))[(((tk>>5)*2+kt)*16+(l>>2))*64+(l&3)*16+c]=u32x4{pk(f[0]*rs,f[1]*rs),pk(f[2]*rs,f[3]*rs),pk(f[4]*rs,f[5]*rs),pk(f[6]*rs,f[7]*rs)};
  }
}
#define PIN(x) asm volatile("":"+v"(x))
#define DL1 8
#define DL2 7
#define DL3 9
#define DL4 0
#define OPT 4
#define STR_(x) #x
#define STR(x) STR_(x)
#define CEXP 0.12751743f
typedef __attribute__((ext_vector_type(16))) u32 u32x16;
typedef __attribute__((ext_vector_type(32))) u32 u32x32;
#define MT(i)
#define MTACC
#define CLOB "v128","v129","v130","v131","v132","v133","v134","v135","v136","v137","v138","v139","v140","v141","v142","v143","v144","v145","v146","v147","v148","v149","v150","v151","v152","v153","v154","v155","v156","v157","v158","v159","v160","v161","v162","v163","v164","v165","v166","v167","v168","v169","v170","v171","v172","v173","v174","v175","v176","v177","v178","v179","v180","v181","v182","v183","v184","v185","v186","v187","v188","v189","v190","v191","v192","v193","v194","v195","v196","v197","v198","v199","v200","v201","v202","v203","v204","v205","v206","v207","v208","v209","v210","v211","v212","v213","v214","v215","v216","v217","v218","v219","v220","v221","v222","v223","v224","v225","v226","v227","v228","v229","v230","v231","v232","v233","v234","v235","v236","v237","v238","v239","v240","v241","v242","v243","v244","v245","v246","v247","s40","s41","s42","s43","s44","s45","s46","s47","s48","s49","s50","s51","s52","s53","s54","s55","s56","s57","s58","s59","s60","s61","s62","s63","s80","s81","s82","s83","s84","s85","s86","s87"
DI void roleR(const Args& A,char* ws,int h,int th,const int w,int& dead){
  const int l=lane(),g=l>>4,n=l&15;
  rsrc_t rw=mk(ws,WS_SZ);
  u32x32 wa[8],wv[2],wo[2];
  { rsrc_t rq=mk(A.wq,4*MB),rk=mk(A.wk,4*MB),rv=mk(A.wv,4*MB),ro=mk(A.wo,4*MB);
    const u32 rl=(32*(g>>1)+8*(g&1))*8192+h*256;
#define ROWJ(j) ((16*(((j)>>1)&1)+4*(((j)>>3)&1)+2*(((j)>>2)&1)+((j)&1))*8192)
    u32x2 v[32]; u32x4 u[16];
    UNR for(int j=0;j<32;j++) v[j]=ld2(rv,rl+n*8,(2*w+(j>>4))*64*8192+th*128+ROWJ(j));
    UNR for(int j=0;j<16;j++) u[j]=ld4(ro,g*8192+n*16,(h*128+th*64+(j&7)+32*(j>>3))*1024+w*256);
    const u32 base=w*32768,va=ROWJ(g)+(32*(n>>3)+8*((n>>2)&1))*8192+h*256+(n&3)*16; u32 ra=base+l*4; PIN(ra); u32x32 f;
    stream<16,8>(base,[&](int c,u32 d){ u32 so=((c+2*w)&7)*64*8192+w*64; asm volatile("":"+s"(so),"+s"(d));
        UNR for(int i=0;i<4;i++) dmaw(c<8?rq:rk,va,so+ROWJ(4*i),d+i*1024); },
      [&](int c,u32 o){ u32 p[16]; UNR for(int i=0;i<16;i++) p[i]=LDS(u32,ra+o+i*256);
        UNR for(int j=0;j<16;j++){ int q=(j&7)*2; f[(c&1)*16+j]=(j&8)?phi(p[q],p[q+1]):plo(p[q],p[q+1]); }
#define PA(i,r) if(c==2*i+1) asm volatile("":"={a[" r "]}"(wa[i]):"0"(f));
        PA(0,"0:31") PA(1,"32:63") PA(2,"64:95") PA(3,"96:127") PA(4,"128:159") PA(5,"160:191") PA(6,"192:223") PA(7,"224:255") });
    bar();
    UNR for(int q=0;q<8;q++) UNR for(int j=0;j<8;j++){ const u32x2* p=v+(q>>2)*16+2*j; int hi=(q>>1)&1;
      wv[q>>2][(q&3)*8+j]=(q&1)?phi(p[0][hi],p[1][hi]):plo(p[0][hi],p[1][hi]); }
    asm volatile("":"+{v[0:31]}"(wv[0]),"+{v[32:63]}"(wv[1])); SB();
    UNR for(int q=0;q<8;q++) UNR for(int j=0;j<8;j++) wo[q>>2][(q&3)*8+j]=(q&1)?phi(u[2*j][q>>1],u[2*j+1][q>>1]):plo(u[2*j][q>>1],u[2*j+1][q>>1]);
    asm volatile("":"+{v[64:95]}"(wo[0]),"+{v[96:127]}"(wo[1]));
  }
  const u32 sel0=__builtin_amdgcn_readfirstlane(ld1(rw,0,O_FL+F_SEL)),sel1=__builtin_amdgcn_readfirstlane(ld1(rw,0,O_FL+F_SEL+4));
  const int pi=2*h+th,p=(n>>1)&1; const u32 l16=l*16;
  { u32x4 sel; UNR for(int j=0;j<4;j++) sel[j]=(n&3)==j?((n&4)?0x3F800000u:0x3F80u):0u; LDS(u32x4,17408+l16)=sel; LDS(u32x4,18432)=u32x4{0x3F803F80u,0x3F803F80u,0x3F80u,0}; }
  const int rho=(2*w+(n>>3))*8+g;
  u32x8 vc={p?0xEEEEu:0x4444u, l16, rho*16384+((pi+rho+(rho&3))&63)*128+(n&7)*16u, 8192+2*g*288+w*64+n*4u,
            8192+(2*(n>>2)+(n&1))*288+g*16u, 32768+l16, w*2048+((g>>1)*4+(n>>2))*128+(n&1)*64+(2*(g&1)+p)*16u,
            w==0?g*64+n*8u:w==3?10496+(4*th+g)*288+n*16u:13312+(w-1)*2048+l16};
  const u32 kvs=O_KV+h*16384+w*3072;
  TS(1) waitf(rw,O_FL+F_KV+2*h*128,1,1,dead); TS(2)
  UNR for(int i=0;i<3;i++) dma16(rw,l16,kvs+i*1024+(3*w+i>=8?th*4096:0),32768+(3*w+i)*1024);
  u32x16 sc={(u32)w, sel0, MB+sel0, 2*MB+sel1, O_M+sel1, pi*16384u, O_KV+h*16384+MB/2+(w-1+(w==3?th:0))*4096, pi*16384u+(w==3?5120:(w-1)*3072),
             O_FL+F_KV+(2*h+1)*128u, O_FL+F_KM+pi*128u, w==3?O_KM+h*2048+th*1024:O_VM+pi*4096+(w-1)*2048u, w==3?65536u:262144u,
             49152+(w-1)*4096u, (u32)dead, 0, 0};
  u32x8 pc={};
  asm volatile(
".macro SQ c,a,w\n"
" v_smfmac_f32_16x16x64_bf16 v[\\c:\\c+3],v[\\a:\\a+3],a[\\w:\\w+7],v248\n"
".endm\n"
".macro SV c,a,w\n"
" v_smfmac_f32_16x16x64_bf16 v[\\c:\\c+3],v[\\a:\\a+3],v[\\w:\\w+7],v248\n"
".endm\n"
".macro MZ d,a,b\n"
" v_mfma_f32_16x16x32_bf16 v[\\d:\\d+3],v[\\a:\\a+3],v[\\b:\\b+3],0\n"
".endm\n"
".macro MA d,a,b\n"
" v_mfma_f32_16x16x32_bf16 v[\\d:\\d+3],v[\\a:\\a+3],v[\\b:\\b+3],v[\\d:\\d+3]\n"
".endm\n"
".macro RD d,a,o\n"
" ds_read_b128 v[\\d:\\d+3],v\\a offset:\\o\n"
".endm\n"
".macro AD2 c\n"
" v_add_f32 v[\\c],v[\\c],v[\\c+2]\n"
" v_add_f32 v[\\c+1],v[\\c+1],v[\\c+3]\n"
".endm\n"
".macro CV d,a,b\n"
" v_cvt_pk_bf16_f32 v[\\d],v[\\a],v[\\b]\n"
".endm\n"
".macro ZR a,n\n"
" .set zi,\\a\n"
" .rept \\n\n"
" v_mov_b64 v[zi:zi+1],0\n"
" .set zi,zi+2\n"
" .endr\n"
".endm\n"
".macro SPIN lab,inc\n"
" s_cmp_eq_u32 s80,0\n"
" s_cselect_b32 s81,\\inc,0\n"
" s_add_u32 s79,s79,s81\n"
" s_add_u32 s80,s80,1\n"
" s_cmp_gt_u32 s80," STR(SPINCAP) "\n"
" s_cselect_b32 s77,1,s77\n"
" s_cmp_lg_u32 s77,0\n"
" s_cbranch_scc1 \\lab\n"
".endm\n"
".macro EXPORT\n"
" RD 144,255,0\n"
" RD 148,255,1024\n"
" s_waitcnt lgkmcnt(0)\n"
" buffer_store_dwordx4 v[144:147],v249,%3,s74 offen sc1\n"
" s_cmp_eq_u32 s64,3\n"
" s_cbranch_scc1 1f\n"
" buffer_store_dwordx4 v[148:151],v249,%3,s74 offen offset:1024 sc1\n"
"1:\n"
".endm\n"
" s_mov_b32 s56,0xCCCCCCCC\n"
" s_mov_b32 s57,0xCCCCCCCC\n"
" s_lshl_b32 s58,s64,11\n"
" s_add_u32 s59,s58,2048\n"
" s_and_b32 s59,s59,8191\n"
" s_xor_b32 s60,s58,4096\n"
" s_xor_b32 s61,s59,4096\n"
" s_lshr_b32 s63,s64,1\n"
" s_lshl_b32 s63,s63,10\n"
" s_and_b32 s82,s64,1\n"
" s_lshl_b32 s82,s82,2\n"
" s_add_u32 s63,s63,s82\n"
" s_mov_b32 s87,0x3e0293ee\n"
" ZR 168,16\n"
" ZR 216,16\n"
" v_mov_b32 v203,0\n"
" s_waitcnt vmcnt(0) lgkmcnt(0)\n"
"RL_loop:\n"
" v_add_u32 v160,s58,v249\n"
" v_add_u32 v161,s59,v249\n"
" v_add_u32 v162,s60,v249\n"
" v_add_u32 v163,s61,v249\n"
" s_add_u32 s84,s65,65536\n"
" v_mov_b32 v202,s78\n"
" v_mov_b64 v[204:205],-1\n"
" v_mov_b64 v[206:207],-1\n"
" s_add_u32 s82,s67,s69\n"
" s_add_u32 s82,s82,s58\n"
" s_mov_b32 s80,0\n"
MT(40)
" buffer_load_dwordx4 v[128:131],v254,%3,s68 offen sc1\n"
" buffer_load_dwordx4 v[132:135],v254,%3,s68 offen offset:1024 sc1\n"
" s_waitcnt vmcnt(0)\n"
"RL_retry:\n"
MT(42)
" ds_write_b128 v160,v[128:131]\n"
" SQ 168,128,0\n"
" ds_write_b128 v160,v[132:135] offset:1024\n"
" v_max3_u32 v200,v128,v129,v130\n"
" SQ 172,128,8\n"
" v_max3_u32 v201,v131,v132,v133\n"
" v_max3_u32 v200,v200,v134,v135\n"
" SQ 168,132,16\n"
" v_max_u32 v200,v200,v201\n"
" v_cmp_eq_u32 vcc,-1,v200\n"
" s_cbranch_vccnz RL_failm\n"
"RL_okm:\n"
" s_waitcnt lgkmcnt(0)\n"
" s_barrier\n"
" RD 136,161,0\n"
" RD 140,161,1024\n"
" SQ 172,132,24\n"
" RD 144,162,0\n"
" RD 148,162,1024\n"
" SQ 176,128,128\n"
" s_cmp_lg_u32 s64,3\n"
" RD 152,163,0\n"
" RD 156,163,1024\n"
" SQ 180,128,136\n"
" s_cbranch_scc1 3f\n"
" buffer_store_dword v202,off,%3,s73 sc1\n"
"3:\n"
" s_mov_b64 vcc,s[56:57]\n"
" s_cmp_eq_u32 s78,63\n"
" SQ 176,132,144\n"
" v_readfirstlane_b32 s85,v203\n"
" SQ 180,132,152\n"
" s_waitcnt lgkmcnt(5)\n"
" SQ 168,136,32\n"
" SQ 172,136,40\n"
" s_waitcnt lgkmcnt(4)\n"
" SQ 168,140,48\n"
" .if " STR(OPT) "&4\n"
" buffer_store_dwordx4 v[204:207],v249,%3,s82 offen sc1\n"
" .endif\n"
" SQ 172,140,56\n"
" s_waitcnt lgkmcnt(3)\n"
" SQ 168,144,64\n"
" SQ 172,144,72\n"
" s_waitcnt lgkmcnt(0)\n"
" SQ 168,148,80\n"
" RD 160,253,2048\n"
" RD 164,253,6144\n"
" SQ 172,148,88\n"
" SQ 168,152,96\n"
" .if " STR(OPT) "&4\n"
" buffer_store_dwordx4 v[204:207],v249,%3,s82 offen offset:1024 sc1\n"
" .endif\n"
" SQ 168,156,112\n"
" SQ 172,152,104\n"
" SQ 172,156,120\n"
" SQ 176,136,160\n"
" AD2 168\n"
" SQ 180,136,168\n"
" RD 136,253,0\n"
" v_add_u32 v170,2304,v251\n"
" SQ 176,140,176\n"
" AD2 172\n"
" SQ 180,140,184\n"
" CV 168,168,172\n"
" CV 169,169,173\n"
" SQ 176,144,192\n"
" ds_write2_b32 v251,v168,v169 offset1:72\n"
" RD 140,253,4096\n"
" SQ 180,144,200\n"
" RD 144,253,1024\n"
" SQ 176,148,208\n"
" SQ 180,148,216\n"
" s_waitcnt lgkmcnt(2)\n"
" s_barrier\n"
" SQ 176,152,224\n"
" RD 148,253,5120\n"
" RD 200,252,0\n"
" SQ 176,156,240\n"
" RD 204,252,64\n"
" RD 208,252,128\n"
" SQ 180,152,232\n"
" RD 212,252,192\n"
" SQ 180,156,248\n"
MT(44)
" SV 184,128,0\n"
" AD2 176\n"
" SV 184,132,32\n"
" RD 152,253,3072\n"
" RD 156,253,7168\n"
" SV 188,128,8\n"
" AD2 180\n"
" SV 188,132,40\n"
" CV 176,176,180\n"
" CV 177,177,181\n"
" SV 192,128,16\n"
" ds_write2_b32 v170,v176,v177 offset1:72\n"
" v_add_u32 v183,s63,v249\n"
" SV 192,132,48\n"
" AD2 184\n"
" SV 196,128,24\n"
" AD2 188\n"
" SV 196,132,56\n"
" s_waitcnt lgkmcnt(0)\n"
" s_barrier\n"
" MZ 168,136,200\n"
" RD 128,252,2304\n"
" RD 132,252,2368\n"
" MZ 172,140,200\n"
" RD 136,252,2432\n"
" RD 140,252,2496\n"
" MA 168,144,204\n"
" CV 184,184,185\n"
" CV 188,188,189\n"
" MA 172,148,204\n"
" AD2 192\n"
" MA 168,160,208\n"
" AD2 196\n"
" MA 172,164,208\n"
" CV 192,192,193\n"
" CV 196,196,197\n"
" MA 168,152,212\n"
" ds_write_b32 v183,v184 offset:13312\n"
" ds_write_b32 v183,v188 offset:15360\n"
" MA 172,156,212\n"
" ds_write_b32 v183,v192 offset:13320\n"
" ds_write_b32 v183,v196 offset:15368\n"
" s_waitcnt lgkmcnt(4)\n"
" MZ 176,128,200\n"
" RD 144,253,8192\n"
" RD 148,253,9216\n"
" MA 176,132,204\n"
" RD 152,253,10240\n"
" RD 156,253,11264\n"
" MA 176,136,208\n"
" v_cndmask_b32 v168,v168,v172,vcc\n"
" v_cndmask_b32 v169,v169,v173,vcc\n"
" MA 176,140,212\n"
" v_cndmask_b32 v170,v170,v174,vcc\n"
" v_cndmask_b32 v171,v171,v175,vcc\n"
" v_max3_f32 v180,v168,v169,v170\n"
" v_max_f32 v180,v180,v171\n"
" s_waitcnt lgkmcnt(4)\n"
" s_barrier\n"
" v_max_f32_dpp v180,v180,v180 quad_perm:[2,3,0,1] row_mask:0xf bank_mask:0xf\n"
" s_cbranch_scc1 RL_last\n"
MT(46)
" v_max3_f32 v181,v180,v176,v177\n"
" v_max3_f32 v180,v180,v176,v177\n"
" v_cndmask_b32 v182,v176,v177,vcc\n"
" RD 196,216,18432\n"
" v_permlane16_swap_b32 v180,v181\n"
" v_max_f32 v183,v180,v181\n"
" v_max_f32 v180,v180,v181\n"
" RD 200,249,13312\n"
" RD 204,249,14336\n"
" v_permlane32_swap_b32 v180,v183\n"
" v_max_f32 v180,v180,v183\n"
" v_mul_f32 v180,s87,v180\n"
" v_fma_f32 v168,v168,s87,-v180\n"
" v_fma_f32 v169,v169,s87,-v180\n"
" v_exp_f32 v168,v168\n"
" v_fma_f32 v170,v170,s87,-v180\n"
" v_exp_f32 v169,v169\n"
" v_fma_f32 v171,v171,s87,-v180\n"
" v_exp_f32 v170,v170\n"
" v_exp_f32 v171,v171\n"
" CV 160,168,169\n"
" CV 161,170,171\n"
" v_cndmask_b32 v172,v160,v216,vcc\n"
" v_cndmask_b32 v173,v161,v216,vcc\n"
" v_cndmask_b32_dpp v174,v160,v216,vcc quad_perm:[2,3,0,1] row_mask:0xf bank_mask:0xf\n"
" v_cndmask_b32_dpp v175,v161,v216,vcc quad_perm:[2,3,0,1] row_mask:0xf bank_mask:0xf\n"
" v_fma_f32 v182,v182,s87,-v180\n"
" s_waitcnt lgkmcnt(0)\n"
" MZ 184,144,172\n"
" v_exp_f32 v182,v182\n"
" v_mov_b32_dpp v164,v172 quad_perm:[2,3,0,1] row_mask:0xf bank_mask:0xf\n"
" MZ 188,148,172\n"
" v_mov_b32_dpp v165,v173 quad_perm:[2,3,0,1] row_mask:0xf bank_mask:0xf\n"
" v_mov_b32_dpp v166,v174 quad_perm:[2,3,0,1] row_mask:0xf bank_mask:0xf\n"
" v_mov_b32_dpp v167,v175 quad_perm:[2,3,0,1] row_mask:0xf bank_mask:0xf\n"
" v_mov_b32_dpp v181,v182 quad_perm:[2,3,0,1] row_mask:0xf bank_mask:0xf\n"
" CV 162,182,181\n"
" MA 184,152,164\n"
" v_cndmask_b32 v168,v162,v216,vcc\n"
" v_cndmask_b32 v169,v162,v216,vcc\n"
" MZ 192,196,160\n"
" v_mov_b32_dpp v170,v168 quad_perm:[2,3,0,1] row_mask:0xf bank_mask:0xf\n"
" v_mov_b32_dpp v171,v168 quad_perm:[2,3,0,1] row_mask:0xf bank_mask:0xf\n"
" MA 188,156,164\n"
" RD 208,249,15360\n"
" RD 212,249,16384\n"
" MA 184,200,168\n"
" MA 184,204,168\n"
" v_add_f32_dpp v192,v192,v192 quad_perm:[2,3,0,1] row_mask:0xf bank_mask:0xf\n"
" s_waitcnt lgkmcnt(1)\n"
" MA 188,208,168\n"
" v_rcp_f32 v192,v192\n"
" s_waitcnt lgkmcnt(0)\n"
" MA 188,212,168\n"
" s_nop 3\n"
" v_mul_f32 v184,v184,v192\n"
" v_mul_f32 v185,v185,v192\n"
" v_mul_f32 v186,v186,v192\n"
" v_mul_f32 v187,v187,v192\n"
" v_mul_f32 v188,v188,v192\n"
" v_mul_f32 v189,v189,v192\n"
" v_mul_f32 v190,v190,v192\n"
" v_mul_f32 v191,v191,v192\n"
" CV 172,184,188\n"
" CV 173,185,189\n"
" CV 174,186,190\n"
" CV 175,187,191\n"
MT(48)
" s_nop 1\n"
" SV 216,172,64\n"
" SV 220,172,72\n"
" SV 224,172,80\n"
" SV 228,172,88\n"
" AD2 216\n"
" CV 128,216,217\n"
" SV 232,172,96\n"
" AD2 220\n"
" CV 129,220,221\n"
" SV 236,172,104\n"
" AD2 224\n"
" CV 130,224,225\n"
" SV 240,172,112\n"
" AD2 228\n"
" CV 131,228,229\n"
" SV 244,172,120\n"
" buffer_store_dwordx4 v[128:131],v250,%3,s65 offen sc1\n"
" AD2 232\n"
" AD2 236\n"
" AD2 240\n"
" CV 132,232,233\n"
" AD2 244\n"
" CV 133,236,237\n"
" CV 134,240,241\n"
" CV 135,244,245\n"
" buffer_store_dwordx4 v[132:135],v250,%3,s84 offen sc1\n"
MT(50)
" RD 160,249,17408\n"
" ZR 168,16\n"
" ZR 216,16\n"
" s_add_u32 s68,s68,16384\n"
" s_cmp_lg_u32 s64,0\n"
" s_cbranch_scc1 RL_other\n"
" s_add_u32 s82,s65,s69\n"
" s_add_u32 s83,s82,4096\n"
" s_lshr_b32 s84,s69,7\n"
" s_add_u32 s84,s84,s68\n"
" s_mov_b32 s80,0\n"
" s_waitcnt lgkmcnt(0)\n"
" s_sleep " STR(DL1) "\n"
MT(52)
"RL_red:\n"
" buffer_load_dwordx4 v[128:131],v249,%3,s82 offen sc1\n"
" buffer_load_dwordx4 v[132:135],v249,%3,s83 offen sc1\n"
" buffer_load_dwordx4 v[136:139],v249,%3,s82 offen offset:1024 sc1\n"
" buffer_load_dwordx4 v[140:143],v249,%3,s83 offen offset:1024 sc1\n"
" buffer_load_dwordx4 v[144:147],v249,%3,s82 offen offset:2048 sc1\n"
" buffer_load_dwordx4 v[148:151],v249,%3,s83 offen offset:2048 sc1\n"
" buffer_load_dwordx4 v[152:155],v249,%3,s82 offen offset:3072 sc1\n"
" buffer_load_dwordx4 v[156:159],v249,%3,s83 offen offset:3072 sc1\n"
" s_waitcnt vmcnt(7)\n"
" MZ 164,160,128\n"
" s_waitcnt vmcnt(6)\n"
" MA 164,160,132\n"
" s_waitcnt vmcnt(5)\n"
" MA 164,160,136\n"
" s_waitcnt vmcnt(4)\n"
" MA 164,160,140\n"
" s_waitcnt vmcnt(3)\n"
" MA 164,160,144\n"
" s_waitcnt vmcnt(2)\n"
" MA 164,160,148\n"
" s_waitcnt vmcnt(1)\n"
" MA 164,160,152\n"
" s_waitcnt vmcnt(0)\n"
" MA 164,160,156\n"
" s_nop 7\n"
" v_cmp_u_f32 vcc,v164,v164\n"
" v_add_f32_dpp v164,v164,v164 row_ror:8 row_mask:0xf bank_mask:0xf\n"
" v_add_f32_dpp v165,v165,v165 row_ror:8 row_mask:0xf bank_mask:0xf\n"
" v_add_f32_dpp v166,v166,v166 row_ror:8 row_mask:0xf bank_mask:0xf\n"
" v_add_f32_dpp v167,v167,v167 row_ror:8 row_mask:0xf bank_mask:0xf\n"
" s_cbranch_vccnz RL_failr\n"
"RL_okr:\n"
" s_mov_b64 exec,0x00FF00FF\n"
" CV 164,164,165\n"
" CV 165,166,167\n"
" buffer_store_dwordx2 v[164:165],v255,%3,s84 offen sc1\n"
" s_mov_b64 exec,-1\n"
"RL_join:\n"
MT(54)
" s_barrier\n"
" s_add_u32 s78,s78,1\n"
" s_mov_b32 s81,s67\n"
" s_mov_b32 s67,s65\n"
" s_mov_b32 s65,s66\n"
" s_mov_b32 s66,s81\n"
" v_xor_b32 v253,16384,v253\n"
MTACC
" s_sleep " STR(DL2) "\n"
" s_branch RL_loop\n"
"RL_other:\n"
" v_mov_b64 v[164:165],-1\n"
" v_mov_b64 v[166:167],-1\n"
" s_add_u32 s82,s67,s71\n"
" s_mov_b32 m0,s76\n"
" s_add_u32 s86,s78,2\n"
" s_min_u32 s86,s86,62\n"
" s_mov_b32 s80,0\n"
" s_cmp_lt_u32 s85,s86\n"
" s_cbranch_scc1 RL_failf\n"
"RL_okf:\n"
" s_xor_b32 s72,s72,128\n"
" s_sleep " STR(DL3) "\n"
" .if (" STR(OPT) "&1)==0\n"
" buffer_load_dwordx4 v249,%3,s70 offen sc1 lds\n"
" buffer_load_dwordx4 v249,%3,s70 offen offset:1024 sc1 lds\n"
" buffer_load_dwordx4 v249,%3,s70 offen offset:2048 sc1 lds\n"
" buffer_load_dwordx4 v249,%3,s70 offen offset:3072 sc1 lds\n"
" .endif\n"
" buffer_load_dword v203,off,%3,s72 sc1\n"
" s_sleep " STR(DL4) "\n"
" .if (" STR(OPT) "&2)==0\n"
" EXPORT\n"
" .endif\n"
" .if (" STR(OPT) "&4)==0\n"
" buffer_store_dwordx4 v[164:167],v249,%3,s82 offen sc1\n"
" buffer_store_dwordx4 v[164:167],v249,%3,s82 offen offset:1024 sc1\n"
" buffer_store_dwordx4 v[164:167],v249,%3,s82 offen offset:2048 sc1\n"
" .endif\n"
" s_add_u32 s70,s70,0x80000\n"
" s_add_u32 s74,s74,s75\n"
" .if " STR(RG) "==7\n"
" s_bitset0_b32 s70,22\n"
" s_lshl_b32 s81,s75,3\n"
" s_andn2_b32 s74,s74,s81\n"
" .endif\n"
" s_xor_b32 s76,s76,16384\n"
" s_branch RL_join\n"
"RL_failm:\n"
" SPIN RL_okm,1\n"
" buffer_load_dwordx4 v[128:131],v254,%3,s68 offen sc1\n"
" buffer_load_dwordx4 v[132:135],v254,%3,s68 offen offset:1024 sc1\n"
" ZR 168,4\n"
" s_waitcnt vmcnt(0)\n"
" s_branch RL_retry\n"
"RL_failr:\n"
" .set zi,129\n"
" .rept 15\n"
" v_max3_u32 v128,v128,v[zi],v[zi+1]\n"
" .set zi,zi+2\n"
" .endr\n"
" v_max_u32 v128,v128,v159\n"
" v_cmp_eq_u32 vcc,-1,v128\n"
" s_cbranch_vccz RL_okr\n"
" SPIN RL_okr,1024\n"
" s_branch RL_red\n"
"RL_failf:\n"
" SPIN RL_okf,0x100000\n"
" buffer_load_dword v200,off,%3,s72 sc1\n"
" s_waitcnt vmcnt(0)\n"
" v_readfirstlane_b32 s85,v200\n"
" s_nop 3\n"
" s_cmp_lt_u32 s85,s86\n"
" s_cbranch_scc1 RL_failf\n"
" s_branch RL_okf\n"
"RL_last:\n"
" s_cmp_eq_u32 s64,0\n"
" s_cbranch_scc1 2f\n"
" s_waitcnt lgkmcnt(0)\n"
" EXPORT\n"
"2:\n"
" v_mov_b32 v202,64\n"
" s_waitcnt vmcnt(0) lgkmcnt(0)\n"
" s_barrier\n"
" buffer_store_dword v202,off,%3,s73 sc1\n"
" s_nop 7\n"
  :"+{s[64:79]}"(sc),"+{v[248:255]}"(vc),"+{s[88:95]}"(pc)
  :"s"(rw),"{a[0:31]}"(wa[0]),"{a[32:63]}"(wa[1]),"{a[64:95]}"(wa[2]),"{a[96:127]}"(wa[3]),"{a[128:159]}"(wa[4]),"{a[160:191]}"(wa[5]),"{a[192:223]}"(wa[6]),"{a[224:255]}"(wa[7]),
   "{v[0:31]}"(wv[0]),"{v[32:63]}"(wv[1]),"{v[64:95]}"(wo[0]),"{v[96:127]}"(wo[1])
  :"memory","scc","vcc",CLOB);
  dead=sc[13];
  TS(3) TC(9,sc[15]&1023) TC(10,(sc[15]>>10)&1023)
}
#define DY 3
#define DW 5
#define GL __attribute__((address_space(1)))
#define XB 0
#define DH 8
#define DZ 24
DI u32 mx4(u32 m,u32x4 v){ return max(max(max(m,v.x),v.y),max(v.z,v.w)); }
DI u32x4 pk8(f32x4 a,f32x4 b){ return u32x4{pk(a.x,b.x),pk(a.y,b.y),pk(a.z,b.z),pk(a.w,b.w)}; }
DI u32x4 pkc(f32x4 a,f32x4 b){ return u32x4{pk(a.x,a.y),pk(a.z,a.w),pk(b.x,b.y),pk(b.z,b.w)}; }
DI void roleB1(const Args& A,char* ws,int h,int par,const int w,int& dead){
  const int l=lane(),g=l>>4,n=l&15;
  rsrc_t rw=mk(ws,WS_SZ); const u32 l16=l*16;
  enum{LA=3,L_B=65536,L_M=139264};
  UNR for(int i=0;i<8;i++) dma16(rw,l16,O_XF+par*32768+(w*8+i)*1024,(w*8+i)*1024);
  u32x4 wq[16][2],wk[16][2],wv[16][2];
  { rsrc_t rk=mk(A.wk,4*MB),rv=mk(A.wv,4*MB),rq=mk(A.wq,4*MB); const u32 base=32768+w*28672;
    const u32 va=g*8192+(n>>2)*65536+(n&3)*16,vb=(l>>5)*8192+((l>>3)&3)*65536+(l&7)*16; u32 ra=base+l*4,rb=base+l*8+(w&1)*4; PIN(ra); PIN(rb);
    stream<32,7>(base,[&](int c,u32 d){ u32 so=h*256; asm volatile("":"+s"(so),"+s"(d));
        UNR for(int i=0;i<4;i++){ if(c>=8&&c<24) dmaw(rv,vb,so+(w>>1)*128+((c-8)*32+2*i)*8192,d+i*1024);
          else dmaw(c<8?rk:rq,va,so+w*64+((c&7)*64+(i>>1)*32+(i&1)*4)*8192,d+i*1024); } },
      [&](int c,u32 o){ u32 t[16]; u32x4 f0,f1;
        if(c>=8&&c<24){ UNR for(int i=0;i<8;i++) t[i]=LDS(u32,rb+o+i*512); mkfr(t,f0,f1);
          if(c<17){ f0=toA(f0); f1=toA(f1); } else { PIN(f0); PIN(f1); } wv[c-8][0]=f0; wv[c-8][1]=f1; }
        else { UNR for(int i=0;i<16;i++) t[i]=LDS(u32,ra+o+i*256);
          UNR for(int q=0;q<2;q++){ mkfr(t+8*q,f0,f1); if(c<8){ wk[2*c+q][0]=toA(f0); wk[2*c+q][1]=toA(f1); } else { PIN(f0); PIN(f1); wq[2*(c&7)+q][0]=f0; wq[2*(c&7)+q][1]=f1; } } } }); }
  int bi=0,cf=0,cg=0; const u32 fo=O_FL+F_O+((par*2+(w&1))*32+h)*128+(w>>1)*4; vm0(); TS(1)
  for(int i=0;i<32+LA;i++){
    const int s=2*i+par,sa=s-2*LA; const u32 lb=L_B+bi*24576+l16; u32x4 pq[2],pk_[2],pv[2];
    bool pub=i>LA; u32 fl=pub?LDS(u32,151552+w*1024+l16):0; bar();
    if(i>=LA){
      if(__any(fl<(u32)sa+1)){ if(pub){ vm0(); st1c(rw,0,fo,sa-1); pub=false; } waitf(rw,O_FL+F_KM+2*h*128,2,sa+1,dead,128); CNT(cf,1) }
      if(i==32) TS(2) if(i==34) TS(3)
    }
#define KMD(j) { if(j==0) dma16(rw,(2*(n>>2)+(n&1))*256+g*16,O_KM+((sa&RG)*32+h)*2048+w*64,L_M+w*1024); \
      else dma16(rw,l16,O_VM+((sa&RG)*32+h)*8192+(2*w+j-1)*1024,L_M+4096+(2*w+j-1)*1024); }
    if(i<32){
      f32x4 cq[2][2]={},ck[2][2]={},cv[2][2]={}; u32 xs=O_XF+(s+2)*32768+w*8192,xd=((i+1)&1)*32768+w*8192;
      u32x4 xx[2][2]; const u32 xb=(i&1)*32768+l16; xx[0][0]=LDS(u32x4,xb); xx[0][1]=LDS(u32x4,xb+16384);
      UNR for(int ks=0;ks<16;ks++){
        UNR for(int kt=0;kt<2;kt++){ u32x4 x=xx[ks&1][kt];
          ck[kt][0]=mfma(wk[ks][0],x,ck[kt][0]); SB(); xx[(ks+1)&1][kt]=LDS(u32x4,xb+(((ks+1)&15)+16*kt)*1024); SB();
          cv[kt][0]=mfma(x,wv[ks][0],cv[kt][0]); ck[kt][1]=mfma(wk[ks][1],x,ck[kt][1]); cv[kt][1]=mfma(x,wv[ks][1],cv[kt][1]); SB(); }
        if(ks<3) KMD(ks) else if(ks<11){ dma16(rw,l16,xs,xd); xs+=1024; xd+=1024; asm volatile("":"+s"(xs),"+s"(xd)); }
        SB(); }
      const u32 gb=O_KV+((s&RG)*32+h)*16384;
      UNR for(int ks=0;ks<16;ks++){ const int j=ks>>3,q=ks&3;
        UNR for(int kt=0;kt<2;kt++){ u32x4 x=xx[ks&1][kt];
          cq[kt][0]=mfma(wq[ks][0],x,cq[kt][0]); SB(); if(ks<15) xx[(ks+1)&1][kt]=LDS(u32x4,xb+(ks+1+16*kt)*1024); SB();
          cq[kt][1]=mfma(wq[ks][1],x,cq[kt][1]); SB();
          if(kt==0){ if(ks&4) pv[j][q]=pk(cv[q>>1][j][(q&1)*2],cv[q>>1][j][(q&1)*2+1]); else pk_[j][q]=pk(ck[j][0][q],ck[j][1][q]); SB(); } }
        if(ks==3){ vm0(); if(pub) st1c(rw,0,fo,sa-1); pub=false; }
        if(q==3){ if(ks&4) st4c(rw,l16,gb+8192+(2*w+j)*1024,pv[j]); else st4c(rw,l16,gb+(j*4+w)*1024,pk_[j]); }
        SB(); }
      UNR for(int j=0;j<2;j++) pq[j]=toA(pk8(cq[j][0],cq[j][1]));

    } else { KMD(0) KMD(1) KMD(2) }
    if(i>=LA){
      const int qt=w&1,eh=w>>1; f32x4 s0={},s1={},sm={}; u32x4 qf[4],kf[4];
      UNR for(int k=0;k<4;k++){ qf[k]=LDS(u32x4,lb+16384+(qt*4+k)*1024); kf[k]=LDS(u32x4,lb+k*1024); } SB();
      UNR for(int k=0;k<4;k++){ s0=mfma(kf[k],qf[k],s0); SB(); kf[k]=LDS(u32x4,lb+(4+k)*1024); SB(); }
      UNR for(int k=0;k<4;k++) s1=mfma(kf[k],qf[k],s1);
      SB(); if(i>=32) vm0();
      if(pub) st1c(rw,0,fo,sa-1);
      dma16(rw,(l16&16)*8,O_FL+F_KM+2*h*128,151552+w*1024); bar();
      UNR for(int k=0;k<4;k++) kf[k]=LDS(u32x4,L_M+k*1024+l16); SB();
      UNR for(int k=0;k<4;k++){ sm=mfma(kf[k],qf[k],sm); SB(); kf[k]=LDS(u32x4,lb+8192+(eh*4+k)*1024); qf[k]=LDS(u32x4,L_M+4096+(eh*4+k)*1024+l16); SB(); }
      const int dq=8*(n>>2)+4*qt+(n&3)-8*g; float pe[10],mx=fmaxf(sm.x,sm.y)*CEXP;
      UNR for(int j=0;j<8;j++){ pe[j]=j<=dq?(j<4?s0[j&3]:s1[j&3])*CEXP:-1.4426950e9f; mx=fmaxf(mx,pe[j]); }
      pe[8]=sm.x*CEXP; pe[9]=sm.y*CEXP; mx=gmax(mx); float su=0;
      UNR for(int j=0;j<10;j++){ pe[j]=__builtin_amdgcn_exp2f(pe[j]-mx); su+=pe[j]; }
      su=__builtin_amdgcn_rcpf(gsum(su));
      u32x4 pf={pk(pe[0],pe[1]),pk(pe[2],pe[3]),pk(pe[4],pe[5]),pk(pe[6],pe[7])}; u32 p1=pk(pe[8],pe[9]); u32x4 pm={p1,p1,p1,p1};
      f32x4 d[4];
      UNR for(int t=0;t<4;t++){ d[t]=mfma(kf[t],pf,f32x4{0,0,0,0});
        d[t]=mfma(u32x4{qf[(t&1)*2][t&2],qf[(t&1)*2][(t&2)+1],qf[(t&1)*2+1][t&2],qf[(t&1)*2+1][(t&2)+1]},pm,d[t])*su; }
      SB(); vm0();
      UNR for(int j=0;j<2;j++) st4c(rw,l16,O_OF+((2*sa+qt)*128+h*4+eh*2+j)*1024,
        u32x4{pk(d[0][2*j],d[1][2*j]),pk(d[2][2*j],d[3][2*j]),pk(d[0][2*j+1],d[1][2*j+1]),pk(d[2][2*j+1],d[3][2*j+1])});
    } else vm0();
    if(i<32){ bar();
      if(w==0) st1c(rw,0,O_FL+F_KV+(2*h+par)*128,s+1);
      UNR for(int j=0;j<2;j++){ LDS(u32x4,lb+(j*4+w)*1024)=pk_[j]; LDS(u32x4,lb+8192+(2*w+j)*1024)=pv[j]; LDS(u32x4,lb+16384+(j*4+w)*1024)=pq[j]; } }
    bi=bi==LA-1?0:bi+1;
  }
  vm0(); st1c(rw,0,fo,64); bar();
  TC(9,cf) TC(10,cg)
}
DI float gelu(float x){ float u=x*(1.f+0.044715f*x*x)*-2.3022082f; return x*__builtin_amdgcn_rcpf(1.f+__builtin_amdgcn_exp2f(u)); }
DI void roleB2(const Args& A,char* ws,int grp,int j,const int w,int& dead){
  const int l=lane(),g=l>>4,n=l&15;
  rsrc_t rw=mk(ws,WS_SZ); const u32 l16=l*16;
  waitf(rw,O_FL+F_KV+(grp*16+j)*128,1,1,dead);
  u32x4 wo[32][2],wi[16][2];
  const int un=j+16*w,isz2=un>=22,u=un-22*isz2;
  { rsrc_t ro=mk(A.wo,4*MB),ri=mk(A.win,512*1376*2); const u32 base=w*32768,va=g*1024+(n>>2)*16384+(n&3)*16,vb=(g+(n>>2)*8)*2752+(n&(u==21?1:3))*16,
      mk_=(u==21&&n>=8)?0:~0u; u32 ra=base+l*4; PIN(ra);
    stream<24,8>(base,[&](int c,u32 d){ u32 so=c<16?j*64+w*MB:un<44?isz2*1376+u*64:0; asm volatile("":"+s"(so),"+s"(d));
        UNR for(int i=0;i<4;i++){ if(c<16) dmaw(ro,va,so+(c*64+i*4)*1024,d+i*1024); else dmaw(ri,vb,so+((c&7)*64+(i>>1)*32+(i&1)*4)*2752,d+i*1024); } },
      [&](int c,u32 o){ u32 t[16]; u32x4 f0,f1; UNR for(int i=0;i<16;i++){ t[i]=LDS(u32,ra+o+i*256); if(c>=16) t[i]&=mk_; }
        UNR for(int q=0;q<2;q++){ mkfr(t+8*q,f0,f1); if(c<15){ f0=toA(f0); f1=toA(f1); } else { PIN(f0); PIN(f1); }
          if(c<16){ wo[2*c+q][0]=f0; wo[2*c+q][1]=f1; } else { wi[2*(c&7)+q][0]=f0; wi[2*(c&7)+q][1]=f1; } } }); }
  { const u32x4 sv={SENT,SENT,SENT,SENT}; const int c=(grp*16+j)*4+w;
    UNR for(int i=0;i<8;i++) st4c(rw,l16+(i&3)*1024,O_HF+(c*8+(i&4))*1024,sv);
    UNR for(int i=0;i<11;i++){ st4c(rw,l16+(i&3)*1024,O_Z1+(c*11+(i&12))*1024,sv); st4c(rw,l16+(i&3)*1024,O_Z2+(c*11+(i&12))*1024,sv); st4c(rw,l16+(i&3)*1024,O_UF+(c*11+(i&12))*1024,sv); }
    UNR for(int i=0;i<2;i++) st4c(rw,l16,O_ST+(c*2+i)*1024,sv);
    vm0(); bar(); if(w==0) st1c(rw,0,O_FL+F_FI+c*32,1); }
  waitf(rw,O_FL+F_FI,64,1,dead,128);
  TS(1)
  enum{L_X=131072,L_P=147456-2048,L_S=153600};
  const u32x4 one={0x3F803F80u,0x3F803F80u,0x3F803F80u,0x3F803F80u}; f32x4 cs0={},cs1={};
  if(!isz2){ UNR for(int k=0;k<16;k++){ cs0=mfma(wi[k][0],one,cs0); cs1=mfma(wi[k][1],one,cs1); } }
  else     { UNR for(int k=0;k<16;k++){ cs0=mfma(one,wi[k][0],cs0); cs1=mfma(one,wi[k][1],cs1); } }
  u32 lx=L_X+l16,lo=w*32768+l16; PIN(lx); PIN(lo); PIN(cs0); PIN(cs1);
  int cf=0,cg=0; rsrc_t rx=mk(A.x,2*MB); const u32 fv=(l&7)*128+(l&8)/2;
  PT0
  for(int it=0;;it++){
    const int blk=grp+4*it,pb=blk-4; const u32 fo=O_FL+F_O+(grp*32+w*8)*128,need=(blk>>1)+1,ob=O_OF+(blk*128+w*32)*1024;
    bool rdy=false;
    if(it){ const int s=pb>>1,kt=pb&1; int sp=0;
      if(w) __builtin_amdgcn_s_sleep(DH+5); else __builtin_amdgcn_s_sleep(DH);
      PT(0) u32x4 x[16]; u32 fl;
      for(;;){ asm volatile("":::"memory"); UNR for(int i=0;i<4;i++) x[i]=ld4c(rw,l16+i*1024,O_HF+(pb*16+w*4)*1024);
        fl=ld1c(rw,fv,fo); u32 mx=0; UNR for(int i=0;i<4;i++) mx=mx4(mx,x[i]);
        if(dead||!__any(mx==SENT)) break; if(++sp>SPINCAP){dead=1;break;} }
      PT(1) UNR for(int i=0;i<4;i++) LDS(u32x4,lx+(w*4+i)*1024)=x[i];
      rdy=!__any(fl<need); bar();
      UNR for(int k=0;k<16;k++) x[k]=LDS(u32x4,lx+k*1024); SB();
      if(it==32) TS(3)
      f32x4 z0={},z1={};
#define OD if((k&3)==0){ SB(); if(rdy) dmaN<1>(rw,l16,ob+k*1024,w*32768+k*1024); SB(); }
      if(w==3){ UNR for(int k=0;k<16;k++){ z0=mfma(x[k],x[k],z0); z1=mfma(one,x[k],z1); OD }
        float mu=z1.x*(1.f/512),q=(n&2)?((n&1)?z0.w:z0.z):((n&1)?z0.y:z0.x);
        if(g==n>>2){ LDS(float,L_S+n*4)=mu; LDS(float,L_S+64+n*4)=rsqrtf(fmaxf(q*(1.f/512)-mu*mu,0.f)+1e-5f); } }
      else if(!isz2){ UNR for(int k=0;k<16;k++){ z0=mfma(wi[k][0],x[k],z0); z1=mfma(wi[k][1],x[k],z1); OD } }
      else     { UNR for(int k=0;k<16;k++){ z0=mfma(x[k],wi[k][0],z0); z1=mfma(x[k],wi[k][1],z1); OD } }
      bar();
      if(un<44){ f32x4 mu,rs;
        if(isz2){ mu=LDS(f32x4,L_S+g*16); rs=LDS(f32x4,L_S+64+g*16); } else { float a=LDS(float,L_S+n*4),b=LDS(float,L_S+64+n*4); mu=f32x4{a,a,a,a}; rs=f32x4{b,b,b,b}; }
        UNR for(int i=0;i<4;i++){ z0[i]=(z0[i]-mu[i]*cs0[i])*rs[i]; z1[i]=(z1[i]-mu[i]*cs1[i])*rs[i]; }
        UNR for(int i=0;i<4;i++){ z0[i]=gelu(z0[i]); z1[i]=gelu(z1[i]); }
        if(!isz2) st4c(rw,l16,O_Z1+(pb*22+u)*1024,pk8(z0,z1));
        else { u32x4 v=pkc(z0,z1); st4c(rw,l16,O_Z2+(s*44+2*u+kt)*1024,v);
          f32x4 a,b; UNR for(int i=0;i<4;i++){ float p=(i&1)?bhi(v[i>>1]):blo(v[i>>1]),q=(i&1)?bhi(v[2+(i>>1)]):blo(v[2+(i>>1)]); a[i]=rsum16(p+q); b[i]=rsum16(p*p+q*q); }
          if(n<2) st4c(rw,g*32+n*16,O_ST+(s*22+u)*256+kt*128,__builtin_bit_cast(u32x4,n?f32x4{a.z,a.w,b.z,b.w}:f32x4{a.x,a.y,b.x,b.y})); } }
      if(it==32){ TC(9,cf) TC(10,cg) PTE break; }
      PT(2)
    }
    if(w==0) dmaw(rx,(8*(n>>2)+(n&3))*1024+g*16,((blk>>1)*32+4*(blk&1))*1024+j*64,154624);
    if(!rdy){ waitf(rw,fo,64,need,dead,0,fv); CNT(cf,1) if(it==31) TS(2) dmaN<4>(rw,l16,ob,w*32768); }
    PT(3) f32x4 c0={},c1={}; u32x4 o[2][4];
    UNR for(int b=0;b<=8;b++){
      if(b<8) asm volatile("s_waitcnt vmcnt(%0)"::"n"(b<4?12:28-4*b):"memory");
      UNR for(int i=0;i<4;i++){ if(b<8) o[b&1][i]=LDS(u32x4,lo+(b*4+i)*1024); SB();
        if(b>0){ c0=mfma(wo[(b-1)*4+i][0],o[(b-1)&1][i],c0); c1=mfma(wo[(b-1)*4+i][1],o[(b-1)&1][i],c1); } SB(); }
      if(b<4) dmaN<1>(rw,l16,ob+16384+b*4096,w*32768+16384+b*4096);
      SB(); }
    PT(4)
    if(w){ LDS(f32x4,L_P+w*2048+l16)=c0; LDS(f32x4,L_P+w*2048+1024+l16)=c1; }
    bar();
    if(w==0){ UNR for(int q=1;q<4;q++){ c0+=LDS(f32x4,L_P+q*2048+l16); c1+=LDS(f32x4,L_P+q*2048+1024+l16); }
      u32x4 a=pk8(c0,c1),hh,xr=LDS(u32x4,154624+l16); UNR for(int i=0;i<4;i++) hh[i]=pk(blo(a[i])+blo(xr[i]),bhi(a[i])+bhi(xr[i]));
      st4c(rw,l16,O_HF+(blk*16+j)*1024,hh); }
    PT(5)
  }
}
DI void roleB3(const Args& A,char* ws,int tb,const int w,int& dead){
  const int l=lane(),g=l>>4,n=l&15;
  rsrc_t rw=mk(ws,WS_SZ),rs_=mk(A.wsp,8*MB); const u32 l16=l*16;
  f32x4 c[12][2]={}; float cr[2]={0,0}; u32x4 z[12],y[12]={}; int cf=0;
  const int tk=tb*32+8*(n>>2)+(n&3); const int nt=w<2?12:10,t0=w<2?12*w:10*w+4;
  UNR for(int i=0;i<DW;i++) __builtin_amdgcn_s_sleep(37);
  for(int i=0;i<32;i++) dma16(rs_,tk*4096+g*16,(w*16+(i>>2)*2+(i&1))*64+(i&2)*8192,((w*16+(i>>2)*2+(i&1))*2+((i>>1)&1))*1024);
  vm0(); TS(1) waitf(rw,O_FL+F_O+tb*128,1,7,dead); waitf(rw,O_FL+F_FI,64,1,dead,128); bar();
  float bsp[2]; UNR for(int kt=0;kt<2;kt++){ bsp[kt]=blo(((const GL unsigned short*)A.bsp)[tk+4*kt]); PIN(bsp[kt]); }
  const u32 v0=n*256+g*32,v1=n<6?v0+4096:0x7FFFF000u,sb=O_ST+(w>>1)*128+(w&1)*16,lw=l16+w*4; u32x4 ta={SENT},tc={};
  for(int s=0;s<64;s++){
    int sp=0; const bool ly=s==max(tb,61);
    while(!dead&&__any(mx4(mx4(0,ta),tc)==SENT)){ if(++sp>SPINCAP){dead=1;break;} if(s<61) __builtin_amdgcn_s_sleep(DZ); else if(sp>1) __builtin_amdgcn_s_sleep(DY); ta=ld4c(rw,v0,sb+s*5632); tc=ld4c(rw,v1,sb+s*5632); }
    f32x4 st=__builtin_bit_cast(f32x4,ta)+__builtin_bit_cast(f32x4,tc); u32 d0=LDS(u32,s*2048+lw),d1=LDS(u32,s*2048+1024+lw); if(s==63) TS(2)
#define LDZ { UNR for(int i=0;i<12;i++) if(i<nt) z[i]=ld4c(rw,l16+(i&3)*1024,O_Z2+(s*44+t0+(i&12))*1024); \
      if(ly){ UNR for(int i=0;i<12;i++) if(i<nt) y[i]=ld4c(rw,l16,O_Z1+((tb*2+(i&1))*22+(t0>>1)+(i>>1))*1024); } }
    SB(); LDZ ta=ld4c(rw,v0,sb+s*5632+5632); tc=ld4c(rw,v1,sb+s*5632+5632); SB();
    UNR for(int i=0;i<4;i++) st[i]=rsum16(st[i])*(1.f/688);
    float ra=rsqrtf(fmaxf(st.z-st.x*st.x,0.f)+1e-5f),rb=rsqrtf(fmaxf(st.w-st.y*st.y,0.f)+1e-5f);
    d0=pk(blo(d0)*ra,bhi(d0)*rb); d1=pk(blo(d1)*ra,bhi(d1)*rb); LDS(u32,s*2048+lw)=d0; LDS(u32,s*2048+1024+lw)=d1;
    cr[0]+=blo(d0)*st.x+bhi(d0)*st.y; cr[1]+=blo(d1)*st.x+bhi(d1)*st.y;
    if(s==63){ UNR for(int kt=0;kt<2;kt++){ float v=gsum(cr[kt]); if(g==0) LDS(float,131072+kt*256+lw)=v; } }
    bar(); const u32x4 b0=LDS(u32x4,s*2048+l16),b1=LDS(u32x4,s*2048+1024+l16); SB();
    for(;;){ u32 mx=0; UNR for(int i=0;i<12;i++) if(i<nt) mx=mx4(mx,z[i]);
      if(ly){ UNR for(int i=0;i<12;i++) if(i<nt) mx=mx4(mx,y[i]); }
      if(dead||!__any(mx==SENT)) break; if(++sp>SPINCAP){dead=1;break;} CNT(cf,1) asm volatile("":::"memory"); LDZ }
    UNR for(int i=0;i<12;i++) if(i<nt){ const int o=(i&1)*2; u32x4 a={z[i&14][o],z[i&14][o+1],z[i|1][o],z[i|1][o+1]}; c[i][0]=mfma(a,b0,c[i][0]); c[i][1]=mfma(a,b1,c[i][1]); }
  }
  TS(3) TC(9,cf)
  UNR for(int kt=0;kt<2;kt++){ f32x4 cc=LDS(f32x4,131072+kt*256+n*16); float bs=bsp[kt]-(cc.x+cc.y+cc.z+cc.w);
    UNR for(int i=0;i<6;i++) if(2*i<nt){ u32x4 v; UNR for(int q=0;q<4;q++) v[q]=pk(blo(y[2*i+kt][q])*(c[2*i][kt][q]+bs),bhi(y[2*i+kt][q])*(c[2*i+1][kt][q]+bs));
      st4c(rw,l16,O_UF+((tb*2+kt)*22+(t0>>1)+i)*1024,v); } }
}
DI void roleF(const Args& A,char* ws,int tb,int nh,const int w,int& dead){
  const int l=lane(),g=l>>4,n=l&15;
  rsrc_t rw=mk(ws,WS_SZ),ro=mk(A.wout,688*1024); const u32 l16=l*16;
  u32x4 wf[22][4];
  { u32x2 t[2][16];
    UNR for(int b=0;b<=11;b++){
      if(b<11){ UNR for(int i=0;i<16;i++) t[b&1][i]=ld2(ro,g*8192+n*8+(b*64+(i>>3)*32+(i&7))*1024,nh*512+w*128); }
      SB();
      if(b>0){ UNR for(int q=0;q<8;q++){ const u32x2* p=&t[(b-1)&1][(q>>2)*8]; int hi=(q>>1)&1; u32x4 f;
          UNR for(int i=0;i<4;i++) f[i]=(q&1)?phi(p[2*i][hi],p[2*i+1][hi]):plo(p[2*i][hi],p[2*i+1][hi]);
          if(b>7) PIN(f); wf[(b-1)*2+(q>>2)][q&3]=b<=7?toA(f):f; } }
      SB(); } }
  waitf(rw,O_FL+F_FI,64,1,dead,128); TS(5)
  const u32 ub=O_UF+(tb*44+w*11)*1024; int sp=0;
  while(!dead&&__any(ld1c(rw,min(n,10)*1024,ub)==SENT)){ if(++sp>SPINCAP){dead=1;break;} asm volatile("s_sleep 2":::"memory"); }
  TS(6)
  u32x4 hf[2][2],uf[11];
  for(;;){ asm volatile("":::"memory"); u32 mx=0;
    UNR for(int i=0;i<11;i++) uf[i]=ld4c(rw,l16+(i&3)*1024,ub+(i&12)*1024);
    UNR for(int i=0;i<4;i++) hf[i>>1][i&1]=ld4c(rw,g*512+(i&1)*256+n*16,O_HF+((tb*2+(i>>1))*16+8*nh+2*w)*1024);
    UNR for(int i=0;i<11;i++){ mx=mx4(mx,uf[i]); LDS(u32x4,(w*11+i)*1024+l16)=uf[i]; }
    UNR for(int i=0;i<4;i++) mx=mx4(mx,hf[i>>1][i&1]);
    if(dead||!__any(mx==SENT)) break; if(++sp>SPINCAP){dead=1;break;} }
  bar();
  f32x4 c[2][4]={};
  u32x4 x[2][4]; UNR for(int i=0;i<4;i++) x[0][i]=LDS(u32x4,((i&1)*22+(i>>1))*1024+l16);
  UNR for(int k=0;k<11;k++){ if(k<10){ UNR for(int i=0;i<4;i++) x[(k+1)&1][i]=LDS(u32x4,((i&1)*22+2*k+2+(i>>1))*1024+l16); } SB();
    UNR for(int i=0;i<4;i++) UNR for(int t=0;t<4;t++) c[i&1][t]=mfma(wf[2*k+(i>>1)][t],x[k&1][i],c[i&1][t]); SB(); }
  UNR for(int kt=0;kt<2;kt++) UNR for(int q=0;q<2;q++){ u32x4 v;
    UNR for(int i=0;i<4;i++){ u32 a=pk(c[kt][(i&1)*2][2*q+(i>>1)],c[kt][(i&1)*2+1][2*q+(i>>1)]); v[i]=pk(blo(a)+blo(hf[kt][q][i]),bhi(a)+bhi(hf[kt][q][i])); }
    ((GL u32x4*)A.out)[(tb*32+8*(n>>2)+4*kt+(n&3))*64+nh*32+w*8+g*2+q]=v; }
}
DI Args AR(){ typedef const __attribute__((address_space(4))) u64* AP; AP p=(AP)__builtin_amdgcn_kernarg_segment_ptr(); asm volatile("":"+s"(p));
  return Args{(void*)p[0],(void*)p[1],(void*)p[2],(void*)p[3],(void*)p[4],(void*)p[5],(void*)p[6],(void*)p[7],(void*)p[8],(void*)p[9],(void*)p[10],(void*)p[11]}; }
__global__ void __launch_bounds__(256,1) main_kernel(Args A){
  __shared__ __attribute__((aligned(1024))) char smem[LDS_SZ];
  asm volatile(""::"v"(smem):"memory");
  char* ws=wsbase(A.ws); const int b=blockIdx.x,x=b&7,y=b>>3; int dead=0; const int w=__builtin_amdgcn_readfirstlane(threadIdx.x>>6);
  TS(0)
  if(y<16){
    if(x<4) roleR(AR(),ws,8*x+(y>>1),y&1,w,dead); else roleB1(AR(),ws,8*(x-4)+(y>>1),y&1,w,dead);
    TS(4) roleF(AR(),ws,8*x+(y>>1),y&1,w,dead);
  }
  else if(x<4) roleB3(AR(),ws,x*16+y-16,w,dead);
  else roleB2(AR(),ws,x-4,y-16,w,dead);
  TS(7)
}
extern "C" void kernel_launch(void* const* d_in,const int* in_sizes,int n_in,void* d_out,int out_size,void* d_ws,size_t ws_size,hipStream_t stream){
  Args A{d_in[0],d_in[1],d_in[2],d_in[3],d_in[4],d_in[5],d_in[8],d_in[9],d_in[10],d_in[11],d_out,d_ws};
  pre_kernel<<<256,256,0,stream>>>(A);
  main_kernel<<<256,256,0,stream>>>(A);
}
```

```cpp
#include <hip/hip_runtime.h>
#include <hip/hip_bf16.h>
typedef unsigned u32;
typedef unsigned long long u64;
typedef __attribute__((ext_vector_type(2))) u32 u32x2;
typedef __attribute__((ext_vector_type(4))) u32 u32x4;
typedef __attribute__((ext_vector_type(8))) u32 u32x8;
typedef __attribute__((ext_vector_type(2))) float f32x2;
typedef __attribute__((ext_vector_type(4))) float f32x4;
typedef __attribute__((ext_vector_type(2))) __bf16 bfx2;
typedef __attribute__((ext_vector_type(8))) __bf16 bfx8;
typedef __amdgpu_buffer_rsrc_t rsrc_t;
#define DI __device__ __forceinline__
#define UNR _Pragma("unroll")
#define SB() __builtin_amdgcn_sched_barrier(0)
#define SENT 0xFFFFFFFFu
#define SPINCAP 400000
#define MB (1u<<20)
#define O_M   (3*MB)
#define O_FL  (4*MB)
#define F_SEL 0
#define F_KV  1024
#define F_KM  (F_KV+8192)
#define F_O   (F_KM+8192)
#define F_RI  (F_O+16384)
#define F_FI  (F_RI+8192)
#define FL_SZ 65536
#define O_XF  (5*MB)
#define O_HF  (7*MB)
#define O_Z1  (9*MB)
#define O_Z2  (12*MB)
#define O_UF  (15*MB)
#define O_ST  (18*MB)
#define O_KM  (20*MB)
#define O_KV  (24*MB)
#define O_VM  (56*MB)
#define O_OF  (72*MB)
#define WS_SZ (88*MB)
#define LDS_SZ (152*1024)

DI rsrc_t mk(const void* p,u32 n){ return __builtin_amdgcn_make_buffer_rsrc((void*)p,0,n,0x00020000); }
DI u32x4 ld4(rsrc_t r,u32 v,u32 s){ return __builtin_amdgcn_raw_buffer_load_b128(r,v,s,0); }
DI u32x2 ld2(rsrc_t r,u32 v,u32 s){ return __builtin_amdgcn_raw_buffer_load_b64(r,v,s,0); }
DI u32   ld1(rsrc_t r,u32 v,u32 s){ return __builtin_amdgcn_raw_buffer_load_b32(r,v,s,0); }
DI u32x4 ld4c(rsrc_t r,u32 v,u32 s){ return __builtin_amdgcn_raw_buffer_load_b128(r,v,s,16); }
DI u32x2 ld2c(rsrc_t r,u32 v,u32 s){ return __builtin_amdgcn_raw_buffer_load_b64(r,v,s,16); }
DI u32   ld1c(rsrc_t r,u32 v,u32 s){ return __builtin_amdgcn_raw_buffer_load_b32(r,v,s,16); }
DI void st4c(rsrc_t r,u32 v,u32 s,u32x4 d){ __builtin_amdgcn_raw_buffer_store_b128(d,r,v,s,16); }
DI void st2c(rsrc_t r,u32 v,u32 s,u32x2 d){ __builtin_amdgcn_raw_buffer_store_b64(d,r,v,s,16); }
DI void st1c(rsrc_t r,u32 v,u32 s,u32 d){ __builtin_amdgcn_raw_buffer_store_b32(d,r,v,s,16); }
DI void dma16(rsrc_t r,u32 v,u32 s,u32 lds){ asm volatile("s_mov_b32 m0, %3\n s_nop 0\n buffer_load_dwordx4 %0, %1, %2 offen sc1 lds"::"v"(v),"s"(r),"s"(s),"s"(lds):"memory"); }
#define SCX "sc1"
template<int N4> DI void dmaN(rsrc_t r,u32 v,u32 s,u32 lds){ asm volatile("s_mov_b32 m0, %3\n .rept %4\n s_nop 0\n .irp o,0,1024,2048,3072\n buffer_load_dwordx4 %1, %2, %0 offen offset:\\o " SCX " lds\n .endr\n s_add_u32 m0,m0,4096\n s_add_u32 %0,%0,4096\n .endr":"+s"(s):"v"(v),"s"(r),"s"(lds),"n"(N4):"memory","scc"); }
DI void vm0(){ asm volatile("s_waitcnt vmcnt(0)":::"memory"); }
DI void bar(){ asm volatile("s_waitcnt lgkmcnt(0)\n s_barrier":::"memory"); }
DI u32 pk(float a,float b){ f32x2 v={a,b}; return __builtin_bit_cast(u32,__builtin_convertvector(v,bfx2)); }
DI float blo(u32 v){ return __uint_as_float(v<<16); }
DI float bhi(u32 v){ return __uint_as_float(v&0xFFFF0000u); }
DI u32 plo(u32 a,u32 b){ return __builtin_amdgcn_perm(b,a,0x05040100u); }
DI u32 phi(u32 a,u32 b){ return __builtin_amdgcn_perm(b,a,0x07060302u); }
DI f32x4 mfma(u32x4 a,u32x4 b,f32x4 c){ return __builtin_amdgcn_mfma_f32_16x16x32_bf16(__builtin_bit_cast(bfx8,a),__builtin_bit_cast(bfx8,b),c,0,0,0); }
DI float gmax(float x){ u32 u=__float_as_uint(x); auto a=__builtin_amdgcn_permlane16_swap(u,u,false,false); x=fmaxf(__uint_as_float(a[0]),__uint_as_float(a[1]));
  u=__float_as_uint(x); auto b=__builtin_amdgcn_permlane32_swap(u,u,false,false); return fmaxf(__uint_as_float(b[0]),__uint_as_float(b[1])); }
DI float gsum(float x){ u32 u=__float_as_uint(x); auto a=__builtin_amdgcn_permlane16_swap(u,u,false,false); x=__uint_as_float(a[0])+__uint_as_float(a[1]);
  u=__float_as_uint(x); auto b=__builtin_amdgcn_permlane32_swap(u,u,false,false); return __uint_as_float(b[0])+__uint_as_float(b[1]); }
template<int C> DI float dppf(float x){ return __int_as_float(__builtin_amdgcn_update_dpp(0,__float_as_int(x),C,0xF,0xF,true)); }
DI float rsum16(float x){ x+=dppf<0x128>(x); x+=dppf<0x124>(x); x+=dppf<0x122>(x); x+=dppf<0x121>(x); return x; }
template<class T> DI T toA(T v){ T o; asm("":"=a"(o):"0"(v)); return o; }
#define LDS(T,off) (*(__attribute__((address_space(3))) T*)(size_t)(off))
DI int lane(){ int l; asm volatile("v_mbcnt_lo_u32_b32 %0, -1, 0\n v_mbcnt_hi_u32_b32 %0, -1, %0":"=v"(l)); __builtin_assume(l>=0&&l<64); return l; }
DI void waitf(rsrc_t r,u32 off,u32 n,u32 need,int& dead,u32 str=4,u32 vo=0){
  if(n>1&&str) vo=(lane()&(n-1))*str; int sp=0;
  while(!dead){ u32 v=ld1c(r,vo,off); if(!__any(v<need)) break; if(++sp>SPINCAP){dead=1;break;} __builtin_amdgcn_s_sleep(2); }
  asm volatile("":::"memory");
}
DI void mkfr(const u32* t,u32x4& f0,u32x4& f1){
  f0=u32x4{plo(t[0],t[1]),plo(t[2],t[3]),plo(t[4],t[5]),plo(t[6],t[7])};
  f1=u32x4{phi(t[0],t[1]),phi(t[2],t[3]),phi(t[4],t[5]),phi(t[6],t[7])};
}
DI void dmaw(rsrc_t r,u32 v,u32 s,u32 lds){ asm volatile("s_mov_b32 m0, %3\n s_nop 0\n buffer_load_dwordx4 %0, %1, %2 offen lds"::"v"(v),"s"(r),"s"(s),"s"(lds):"memory"); }
template<int NC,int NR,class I,class U> DI void stream(u32 base,I issue,U use){
  UNR for(int c=0;c<NR;c++) issue(c,base+c*4096);
  UNR for(int c=0;c<NC;c++){ asm volatile("s_waitcnt vmcnt(%0)"::"n"(4*((NC-c<NR?NC-c:NR)-1)):"memory");
    use(c,(c%NR)*4096); SB(); asm volatile("s_waitcnt lgkmcnt(0)":::"memory");
    if(c+NR<NC) issue(c+NR,base+(c%NR)*4096); SB(); }
}
#define TC(i,v)
#define CNT(c,v)
#define PT(i)
#define PT0
#define PTE
#define TS(i) {}
#undef PT
#undef PT0
#undef PTE
#define PT(i)
#define PT0
#define PTE
struct Args{ const void *x,*mem0,*wq,*wk,*wv,*wo,*win,*wsp,*bsp,*wout; void *out,*ws; };
DI char* wsbase(void* ws){ return (char*)(((size_t)ws+(2*MB-1))&~(size_t)(2*MB-1)); }

#define NCAL 4
#define PSC 0
__global__ void __launch_bounds__(256) pre_kernel(Args A){
  char* ws=wsbase(A.ws); rsrc_t rw=mk(ws,WS_SZ);
  int b=blockIdx.x,t=threadIdx.x,l=t&63,w=__builtin_amdgcn_readfirstlane(t>>6);
  __shared__ u32 tm[8];
  u32x4 xv[2]; TS(22)
  const bool cal=b<256&&(b&127)==0; const int br=b<256?b:(b&1)*128;
  if(!cal){ UNR for(int i=0;i<2;i++) xv[i]=((const u32x4*)A.x)[(br*8+w*2+i)*64+l]; }
  else {
    if(l<4) st1c(rw,(l&1)*8192+(l>>1)*1024,b*16384+w*2048,SENT);
    vm0(); u32 mn[2]={~0u,~0u};
    UNR for(int i=0;i<NCAL;i++){ bar();
      u64 c0=__builtin_amdgcn_s_memtime(); asm volatile("s_waitcnt lgkmcnt(0)":::"memory");
      u32 v=ld1c(rw,(i&1)*8192+(i>>1)*1024,b*16384+w*2048); asm volatile("s_waitcnt vmcnt(0)":"+v"(v)::"memory");
      u64 c1=__builtin_amdgcn_s_memtime(); mn[i&1]=min(mn[i&1],(u32)(c1-c0)); }
    if(l<2) tm[l*4+w]=l?mn[1]:mn[0];
    __syncthreads();
    if(t==0) *(u32*)(ws+O_FL+F_SEL+(b>>7)*4)=min(min(tm[4],tm[5]),min(tm[6],tm[7]))<min(min(tm[0],tm[1]),min(tm[2],tm[3]))?8192u:0u;
  }
  if(b==192)
  UNR for(int i=0;i<4;i++){ u32x4 v={SENT,SENT,SENT,SENT}; int c=i*256+t;
    { int cc=c&511; const u32x2* p=(const u32x2*)A.mem0+((cc>>2)&7)*128+(cc>>6)*16+(cc&3)*4+((cc>>5)&1); u32x2 a=p[0],e=p[2]; v=u32x4{a.x,a.y,e.x,e.y}; }
    __builtin_amdgcn_raw_buffer_store_b128(v,rw,(b*1024+c)*16,0,PSC); }
  if(b>=4&&b<256&&t<16) ((u32x4*)(ws+O_FL))[b*16+t]=u32x4{0,0,0,0};
  if(!cal) UNR for(int i=0;i<2;i++){
    float f[8],s=0; UNR for(int j=0;j<4;j++){ f[2*j]=blo(xv[i][j]); f[2*j+1]=bhi(xv[i][j]); s+=f[2*j]+f[2*j+1]; }
    float mu=gsum(rsum16(s))*(1.f/512),q=0; UNR for(int j=0;j<8;j++){ f[j]-=mu; q+=f[j]*f[j]; }
    float rs=rsqrtf(gsum(rsum16(q))*(1.f/512)+1e-5f);
    int tk=br*8+w*2+i,tau=tk&31,kt=(tau>>2)&1,c=4*(tau>>3)+(tau&3);
    ((u32x4*)(ws+O_XF))[(((tk>>5)*2+kt)*16+(l>>2))*64+(l&3)*16+c]=u32x4{pk(f[0]*rs,f[1]*rs),pk(f[2]*rs,f[3]*rs),pk(f[4]*rs,f[5]*rs),pk(f[6]*rs,f[7]*rs)};
  }
  TS(23)
}
#define RG 63
#define PIN(x) asm volatile("":"+v"(x))
#define DL1 10
#define DL2 7
#define DL3 0
#define DL4 0
#define OPT 4
#define STR_(x) #x
#define STR(x) STR_(x)
#define CEXP 0.12751743f
typedef __attribute__((ext_vector_type(16))) u32 u32x16;
typedef __attribute__((ext_vector_type(32))) u32 u32x32;
#define MT(i)
#define MTACC
#define CLOB "v128","v129","v130","v131","v132","v133","v134","v135","v136","v137","v138","v139","v140","v141","v142","v143","v144","v145","v146","v147","v148","v149","v150","v151","v152","v153","v154","v155","v156","v157","v158","v159","v160","v161","v162","v163","v164","v165","v166","v167","v168","v169","v170","v171","v172","v173","v174","v175","v176","v177","v178","v179","v180","v181","v182","v183","v184","v185","v186","v187","v188","v189","v190","v191","v192","v193","v194","v195","v196","v197","v198","v199","v200","v201","v202","v203","v204","v205","v206","v207","v208","v209","v210","v211","v212","v213","v214","v215","v216","v217","v218","v219","v220","v221","v222","v223","v224","v225","v226","v227","v228","v229","v230","v231","v232","v233","v234","v235","v236","v237","v238","v239","v240","v241","v242","v243","v244","v245","v246","v247","s40","s41","s42","s43","s44","s45","s46","s47","s48","s49","s50","s51","s52","s53","s54","s55","s56","s57","s58","s59","s60","s61","s62","s63","s80","s81","s82","s83","s84","s85","s86","s87"
DI void roleR(const Args& A,char* ws,int h,int th,const int w,int& dead){
  const int l=lane(),g=l>>4,n=l&15;
  rsrc_t rw=mk(ws,WS_SZ);
  u32x32 wa[8],wv[2],wo[2];
  { rsrc_t rq=mk(A.wq,4*MB),rk=mk(A.wk,4*MB),rv=mk(A.wv,4*MB),ro=mk(A.wo,4*MB);
    const u32 rl=(32*(g>>1)+8*(g&1))*8192+h*256;
#define ROWJ(j) ((16*(((j)>>1)&1)+4*(((j)>>3)&1)+2*(((j)>>2)&1)+((j)&1))*8192)
    u32x2 v[32]; u32x4 u[16];
    UNR for(int j=0;j<32;j++) v[j]=ld2(rv,rl+n*8,(2*w+(j>>4))*64*8192+th*128+ROWJ(j));
    UNR for(int j=0;j<16;j++) u[j]=ld4(ro,g*8192+n*16,(h*128+th*64+(j&7)+32*(j>>3))*1024+w*256);
    const u32 base=w*32768,va=ROWJ(g)+(32*(n>>3)+8*((n>>2)&1))*8192+h*256+(n&3)*16; u32 ra=base+l*4; PIN(ra); u32x32 f;
    stream<16,8>(base,[&](int c,u32 d){ u32 so=((c+2*w)&7)*64*8192+w*64; asm volatile("":"+s"(so),"+s"(d));
        UNR for(int i=0;i<4;i++) dmaw(c<8?rq:rk,va,so+ROWJ(4*i),d+i*1024); },
      [&](int c,u32 o){ u32 p[16]; UNR for(int i=0;i<16;i++) p[i]=LDS(u32,ra+o+i*256);
        UNR for(int j=0;j<16;j++){ int q=(j&7)*2; f[(c&1)*16+j]=(j&8)?phi(p[q],p[q+1]):plo(p[q],p[q+1]); }
#define PA(i,r) if(c==2*i+1) asm volatile("":"={a[" r "]}"(wa[i]):"0"(f));
        PA(0,"0:31") PA(1,"32:63") PA(2,"64:95") PA(3,"96:127") PA(4,"128:159") PA(5,"160:191") PA(6,"192:223") PA(7,"224:255") });
    bar();
    UNR for(int q=0;q<8;q++) UNR for(int j=0;j<8;j++){ const u32x2* p=v+(q>>2)*16+2*j; int hi=(q>>1)&1;
      wv[q>>2][(q&3)*8+j]=(q&1)?phi(p[0][hi],p[1][hi]):plo(p[0][hi],p[1][hi]); }
    asm volatile("":"+{v[0:31]}"(wv[0]),"+{v[32:63]}"(wv[1])); SB();
    UNR for(int q=0;q<8;q++) UNR for(int j=0;j<8;j++) wo[q>>2][(q&3)*8+j]=(q&1)?phi(u[2*j][q>>1],u[2*j+1][q>>1]):plo(u[2*j][q>>1],u[2*j+1][q>>1]);
    asm volatile("":"+{v[64:95]}"(wo[0]),"+{v[96:127]}"(wo[1]));
  }
  const u32 sel0=__builtin_amdgcn_readfirstlane(ld1(rw,0,O_FL+F_SEL)),sel1=__builtin_amdgcn_readfirstlane(ld1(rw,0,O_FL+F_SEL+4));
  const int pi=2*h+th,p=(n>>1)&1; const u32 l16=l*16;
  { u32x4 sel; UNR for(int j=0;j<4;j++) sel[j]=(n&3)==j?((n&4)?0x3F800000u:0x3F80u):0u; LDS(u32x4,17408+l16)=sel; LDS(u32x4,18432)=u32x4{0x3F803F80u,0x3F803F80u,0x3F80u,0}; }
  const int rho=(2*w+(n>>3))*8+g;
  u32x8 vc={p?0xEEEEu:0x4444u, l16, rho*16384+((pi+rho+(rho&3))&63)*128+(n&7)*16u, 8192+2*g*288+w*64+n*4u,
            8192+(2*(n>>2)+(n&1))*288+g*16u, 32768+l16, w*2048+((g>>1)*4+(n>>2))*128+(n&1)*64+(2*(g&1)+p)*16u,
            w==0?g*64+n*8u:w==3?10496+(4*th+g)*288+n*16u:13312+(w-1)*2048+l16};
  const u32 kvs=O_KV+h*16384+w*3072;
  TS(1) waitf(rw,O_FL+F_RI,64,1,dead,128); waitf(rw,O_FL+F_KV+2*h*128,1,1,dead); TS(2) TC(20,__builtin_amdgcn_s_memtime())
  UNR for(int i=0;i<3;i++) dma16(rw,l16,kvs+i*1024+(3*w+i>=8?th*4096:0),32768+(3*w+i)*1024);
  u32x16 sc={(u32)w, sel0, MB+sel0, 2*MB+sel1, O_M+sel1, pi*16384u, O_KV+h*16384+MB/2+(w-1+(w==3?th:0))*4096, pi*16384u+(w==3?5120:(w-1)*3072),
             O_FL+F_KV+(2*h+1)*128u, O_FL+F_KM+pi*128u, w==3?O_KM+h*2048+th*1024:O_VM+pi*4096+(w-1)*2048u, w==3?65536u:262144u,
             49152+(w-1)*4096u, (u32)dead, 0, 0};
  u32x8 pc={};
  asm volatile(
".macro SQ c,a,w\n"
" v_smfmac_f32_16x16x64_bf16 v[\\c:\\c+3],v[\\a:\\a+3],a[\\w:\\w+7],v248\n"
".endm\n"
".macro SV c,a,w\n"
" v_smfmac_f32_16x16x64_bf16 v[\\c:\\c+3],v[\\a:\\a+3],v[\\w:\\w+7],v248\n"
".endm\n"
".macro MZ d,a,b\n"
" v_mfma_f32_16x16x32_bf16 v[\\d:\\d+3],v[\\a:\\a+3],v[\\b:\\b+3],0\n"
".endm\n"
".macro MA d,a,b\n"
" v_mfma_f32_16x16x32_bf16 v[\\d:\\d+3],v[\\a:\\a+3],v[\\b:\\b+3],v[\\d:\\d+3]\n"
".endm\n"
".macro RD d,a,o\n"
" ds_read_b128 v[\\d:\\d+3],v\\a offset:\\o\n"
".endm\n"
".macro AD2 c\n"
" v_add_f32 v[\\c],v[\\c],v[\\c+2]\n"
" v_add_f32 v[\\c+1],v[\\c+1],v[\\c+3]\n"
".endm\n"
".macro CV d,a,b\n"
" v_cvt_pk_bf16_f32 v[\\d],v[\\a],v[\\b]\n"
".endm\n"
".macro ZR a,n\n"
" .set zi,\\a\n"
" .rept \\n\n"
" v_mov_b64 v[zi:zi+1],0\n"
" .set zi,zi+2\n"
" .endr\n"
".endm\n"
".macro SPIN lab,inc\n"
" s_cmp_eq_u32 s80,0\n"
" s_cselect_b32 s81,\\inc,0\n"
" s_add_u32 s79,s79,s81\n"
" s_add_u32 s80,s80,1\n"
" s_cmp_gt_u32 s80," STR(SPINCAP) "\n"
" s_cselect_b32 s77,1,s77\n"
" s_cmp_lg_u32 s77,0\n"
" s_cbranch_scc1 \\lab\n"
".endm\n"
".macro EXPORT\n"
" RD 144,255,0\n"
" RD 148,255,1024\n"
" s_waitcnt lgkmcnt(0)\n"
" buffer_store_dwordx4 v[144:147],v249,%3,s74 offen sc1\n"
" s_cmp_eq_u32 s64,3\n"
" s_cbranch_scc1 1f\n"
" buffer_store_dwordx4 v[148:151],v249,%3,s74 offen offset:1024 sc1\n"
"1:\n"
".endm\n"
" s_mov_b32 s56,0xCCCCCCCC\n"
" s_mov_b32 s57,0xCCCCCCCC\n"
" s_lshl_b32 s58,s64,11\n"
" s_add_u32 s59,s58,2048\n"
" s_and_b32 s59,s59,8191\n"
" s_xor_b32 s60,s58,4096\n"
" s_xor_b32 s61,s59,4096\n"
" s_lshr_b32 s63,s64,1\n"
" s_lshl_b32 s63,s63,10\n"
" s_and_b32 s82,s64,1\n"
" s_lshl_b32 s82,s82,2\n"
" s_add_u32 s63,s63,s82\n"
" s_mov_b32 s87,0x3e0293ee\n"
" ZR 168,16\n"
" ZR 216,16\n"
" v_mov_b32 v203,0\n"
" s_waitcnt vmcnt(0) lgkmcnt(0)\n"
"RL_loop:\n"
" v_add_u32 v160,s58,v249\n"
" v_add_u32 v161,s59,v249\n"
" v_add_u32 v162,s60,v249\n"
" v_add_u32 v163,s61,v249\n"
" s_add_u32 s84,s65,65536\n"
" s_mov_b32 m0,s76\n"
" s_add_u32 s86,s78,2\n"
" s_min_u32 s86,s86,62\n"
" s_cmp_eq_u32 s64,0\n"
" s_cselect_b32 s86,0,s86\n"
" v_mov_b32 v202,s78\n"
" v_mov_b64 v[204:205],-1\n"
" v_mov_b64 v[206:207],-1\n"
" s_add_u32 s82,s67,s69\n"
" s_add_u32 s82,s82,s58\n"
" s_mov_b32 s80,0\n"
MT(40)
" buffer_load_dwordx4 v[128:131],v254,%3,s68 offen sc1\n"
" buffer_load_dwordx4 v[132:135],v254,%3,s68 offen offset:1024 sc1\n"
" s_waitcnt vmcnt(0)\n"
"RL_retry:\n"
MT(42)
" ds_write_b128 v160,v[128:131]\n"
" SQ 168,128,0\n"
" ds_write_b128 v160,v[132:135] offset:1024\n"
" v_max3_u32 v200,v128,v129,v130\n"
" SQ 172,128,8\n"
" v_max3_u32 v201,v131,v132,v133\n"
" v_max3_u32 v200,v200,v134,v135\n"
" SQ 168,132,16\n"
" v_max_u32 v200,v200,v201\n"
" v_cmp_eq_u32 vcc,-1,v200\n"
" s_cbranch_vccnz RL_failm\n"
"RL_okm:\n"
" SV 184,128,0\n"
" v_add_u32 v201,s63,v249\n"
" SV 184,132,32\n"
" v_readfirstlane_b32 s85,v203\n"
" SV 188,128,8\n"
" SV 188,132,40\n"
" SV 192,128,16\n"
" AD2 184\n"
" SV 192,132,48\n"
" CV 184,184,185\n"
" SV 196,128,24\n"
" AD2 188\n"
" SV 196,132,56\n"
" CV 188,188,189\n"
" s_cmp_lg_u32 s64,3\n"
" s_waitcnt lgkmcnt(0)\n"
" s_barrier\n"
" RD 136,161,0\n"
" RD 140,161,1024\n"
" SQ 172,132,24\n"
" RD 144,162,0\n"
" SQ 176,128,128\n"
" RD 148,162,1024\n"
" SQ 180,128,136\n"
" RD 152,163,0\n"
" SQ 176,132,144\n"
" RD 156,163,1024\n"
" SQ 180,132,152\n"
" s_cbranch_scc1 3f\n"
" buffer_store_dword v202,off,%3,s73 sc1\n"
"3:\n"
" AD2 192\n"
" s_waitcnt lgkmcnt(5)\n"
" SQ 168,136,32\n"
" AD2 196\n"
" SQ 172,136,40\n"
" CV 192,192,193\n"
" CV 196,196,197\n"
" s_waitcnt lgkmcnt(0)\n"
" SQ 168,140,48\n"
" .if (" STR(OPT) "&20)==4\n"
" buffer_store_dwordx4 v[204:207],v249,%3,s82 offen sc1\n"
" .endif\n"
" ds_write_b32 v201,v184 offset:13312\n"
" SQ 172,140,56\n"
" .if " STR(OPT) "&8\n"
" s_cmp_lt_u32 s85,s86\n"
" s_cbranch_scc1 RL_failf\n"
"RL_okf:\n"
" s_cmp_eq_u32 s64,0\n"
" .endif\n"
" ds_write_b32 v201,v188 offset:15360\n"
" SQ 168,144,64\n"
" ds_write_b32 v201,v192 offset:13320\n"
" SQ 172,144,72\n"
" .if " STR(OPT) "&8\n"
" s_cbranch_scc1 5f\n"
" buffer_load_dwordx4 v249,%3,s70 offen offset:0 sc1 lds\n"
"5:\n"
" .endif\n"
" RD 128,253,0\n"
" SQ 168,148,80\n"
" .if (" STR(OPT) "&20)==4\n"
" buffer_store_dwordx4 v[204:207],v249,%3,s82 offen offset:1024 sc1\n"
" .endif\n"
" RD 132,253,4096\n"
" SQ 172,148,88\n"
" ds_write_b32 v201,v196 offset:15368\n"
" SQ 168,152,96\n"
" .if " STR(OPT) "&8\n"
" s_cbranch_scc1 5f\n"
" buffer_load_dwordx4 v249,%3,s70 offen offset:1024 sc1 lds\n"
"5:\n"
" .endif\n"
" RD 160,253,1024\n"
" SQ 168,156,112\n"
" RD 164,253,5120\n"
" SQ 172,152,104\n"
" .if " STR(OPT) "&8\n"
" s_cbranch_scc1 5f\n"
" buffer_load_dwordx4 v249,%3,s70 offen offset:2048 sc1 lds\n"
"5:\n"
" .endif\n"
" RD 184,253,2048\n"
" SQ 172,156,120\n"
" RD 188,253,6144\n"
" SQ 176,136,160\n"
" AD2 168\n"
" SQ 180,136,168\n"
" .if " STR(OPT) "&8\n"
" s_cbranch_scc1 5f\n"
" buffer_load_dwordx4 v249,%3,s70 offen offset:3072 sc1 lds\n"
"5:\n"
" .endif\n"
" RD 192,253,3072\n"
" SQ 176,140,176\n"
" AD2 172\n"
" SQ 180,140,184\n"
" CV 168,168,172\n"
" CV 169,169,173\n"
" SQ 176,144,192\n"
" ds_write2_b32 v251,v168,v169 offset1:72\n"
" SQ 180,144,200\n"
" RD 196,253,7168\n"
" SQ 176,148,208\n"
" .if " STR(OPT) "&16\n"
" buffer_store_dwordx4 v[204:207],v249,%3,s82 offen sc1\n"
" .endif\n"
" SQ 180,148,216\n"
" .if " STR(OPT) "&16\n"
" buffer_store_dwordx4 v[204:207],v249,%3,s82 offen offset:1024 sc1\n"
" .endif\n"
" s_waitcnt lgkmcnt(1)\n"
" s_barrier\n"
" RD 200,252,0\n"
" SQ 176,152,224\n"
" RD 204,252,64\n"
" SQ 176,156,240\n"
" RD 208,252,128\n"
" SQ 180,152,232\n"
" RD 212,252,192\n"
" s_mov_b64 vcc,s[56:57]\n"
" SQ 180,156,248\n"
MT(44)
" s_cmp_eq_u32 s78,63\n"
" AD2 176\n"
" s_waitcnt lgkmcnt(3)\n"
" MZ 168,128,200\n"
" MZ 172,132,200\n"
" s_waitcnt lgkmcnt(2)\n"
" AD2 180\n"
" MA 168,160,204\n"
" CV 176,176,180\n"
" CV 177,177,181\n"
" ds_write_b32 v251,v176 offset:2304\n"
" ds_write_b32 v251,v177 offset:2592\n"
" MA 172,164,204\n"
" s_waitcnt lgkmcnt(2)\n"
" MA 168,184,208\n"
" MA 172,188,208\n"
" s_waitcnt lgkmcnt(0)\n"
" s_barrier\n"
" s_cbranch_scc1 RL_last\n"
" RD 136,252,2304\n"
" RD 140,252,2368\n"
" MA 168,192,212\n"
" RD 144,252,2432\n"
" RD 148,252,2496\n"
" MA 172,196,212\n"
" RD 152,253,8192\n"
" RD 156,253,9216\n"
" RD 128,253,10240\n"
" RD 132,253,11264\n"
" RD 196,216,18432\n"
" s_waitcnt lgkmcnt(8)\n"
" MZ 176,136,200\n"
" s_nop 0\n"
" v_cndmask_b32 v168,v168,v172,vcc\n"
" v_cndmask_b32 v169,v169,v173,vcc\n"
" s_waitcnt lgkmcnt(7)\n"
" MA 176,140,204\n"
" v_cndmask_b32 v170,v170,v174,vcc\n"
" v_cndmask_b32 v171,v171,v175,vcc\n"
" s_waitcnt lgkmcnt(5)\n"
" MA 176,144,208\n"
" v_max3_f32 v180,v168,v169,v170\n"
" v_max_f32 v180,v180,v171\n"
" MA 176,148,212\n"
" RD 200,249,13312\n"
" RD 204,249,14336\n"
" v_max_f32_dpp v180,v180,v180 quad_perm:[2,3,0,1] row_mask:0xf bank_mask:0xf\n"
" RD 208,249,15360\n"
" RD 212,249,16384\n"
" s_nop 2\n"
MT(46)
" v_max3_f32 v181,v180,v176,v177\n"
" v_max3_f32 v180,v180,v176,v177\n"
" v_cndmask_b32 v182,v176,v177,vcc\n"
" s_nop 0\n"
" v_permlane16_swap_b32 v180,v181\n"
" v_max_f32 v183,v180,v181\n"
" v_max_f32 v180,v180,v181\n"
" s_nop 1\n"
" v_permlane32_swap_b32 v180,v183\n"
" v_max_f32 v180,v180,v183\n"
" v_mul_f32 v180,s87,v180\n"
" v_fma_f32 v168,v168,s87,-v180\n"
" v_fma_f32 v169,v169,s87,-v180\n"
" v_exp_f32 v168,v168\n"
" v_fma_f32 v170,v170,s87,-v180\n"
" v_exp_f32 v169,v169\n"
" v_fma_f32 v171,v171,s87,-v180\n"
" v_exp_f32 v170,v170\n"
" v_exp_f32 v171,v171\n"
" CV 160,168,169\n"
" CV 161,170,171\n"
" v_cndmask_b32 v172,v160,v216,vcc\n"
" v_cndmask_b32 v173,v161,v216,vcc\n"
" v_cndmask_b32_dpp v174,v160,v216,vcc quad_perm:[2,3,0,1] row_mask:0xf bank_mask:0xf\n"
" v_cndmask_b32_dpp v175,v161,v216,vcc quad_perm:[2,3,0,1] row_mask:0xf bank_mask:0xf\n"
" v_fma_f32 v182,v182,s87,-v180\n"
" s_waitcnt lgkmcnt(0)\n"
" MZ 184,152,172\n"
" v_exp_f32 v182,v182\n"
" v_mov_b32_dpp v164,v172 quad_perm:[2,3,0,1] row_mask:0xf bank_mask:0xf\n"
" MZ 188,156,172\n"
" v_mov_b32_dpp v165,v173 quad_perm:[2,3,0,1] row_mask:0xf bank_mask:0xf\n"
" v_mov_b32_dpp v166,v174 quad_perm:[2,3,0,1] row_mask:0xf bank_mask:0xf\n"
" v_mov_b32_dpp v167,v175 quad_perm:[2,3,0,1] row_mask:0xf bank_mask:0xf\n"
" v_mov_b32_dpp v181,v182 quad_perm:[2,3,0,1] row_mask:0xf bank_mask:0xf\n"
" CV 162,182,181\n"
" MA 184,128,164\n"
" v_cndmask_b32 v168,v162,v216,vcc\n"
" v_cndmask_b32 v169,v162,v216,vcc\n"
" MZ 192,196,160\n"
" v_mov_b32_dpp v170,v168 quad_perm:[2,3,0,1] row_mask:0xf bank_mask:0xf\n"
" v_mov_b32_dpp v171,v168 quad_perm:[2,3,0,1] row_mask:0xf bank_mask:0xf\n"
" MA 188,132,164\n"
" s_nop 0\n"
" MA 184,200,168\n"
" MA 184,204,168\n"
" v_add_f32_dpp v192,v192,v192 quad_perm:[2,3,0,1] row_mask:0xf bank_mask:0xf\n"
" MA 188,208,168\n"
" v_rcp_f32 v192,v192\n"
" MA 188,212,168\n"
" s_nop 3\n"
" v_mul_f32 v184,v184,v192\n"
" v_mul_f32 v185,v185,v192\n"
" v_mul_f32 v186,v186,v192\n"
" v_mul_f32 v187,v187,v192\n"
" v_mul_f32 v188,v188,v192\n"
" v_mul_f32 v189,v189,v192\n"
" v_mul_f32 v190,v190,v192\n"
" v_mul_f32 v191,v191,v192\n"
" CV 172,184,188\n"
" CV 173,185,189\n"
" CV 174,186,190\n"
" CV 175,187,191\n"
MT(48)
" s_nop 1\n"
" SV 216,172,64\n"
" SV 220,172,72\n"
" SV 224,172,80\n"
" SV 228,172,88\n"
" AD2 216\n"
" CV 128,216,217\n"
" SV 232,172,96\n"
" AD2 220\n"
" CV 129,220,221\n"
" SV 236,172,104\n"
" AD2 224\n"
" CV 130,224,225\n"
" SV 240,172,112\n"
" AD2 228\n"
" CV 131,228,229\n"
" SV 244,172,120\n"
" buffer_store_dwordx4 v[128:131],v250,%3,s65 offen sc1\n"
" AD2 232\n"
" AD2 236\n"
" AD2 240\n"
" CV 132,232,233\n"
" AD2 244\n"
" CV 133,236,237\n"
" CV 134,240,241\n"
" CV 135,244,245\n"
" buffer_store_dwordx4 v[132:135],v250,%3,s84 offen sc1\n"
MT(50)
" RD 160,249,17408\n"
" ZR 168,16\n"
" ZR 216,16\n"
" s_add_u32 s68,s68,16384\n"
" s_cmp_lg_u32 s64,0\n"
" s_cbranch_scc1 RL_other\n"
" s_add_u32 s82,s65,s69\n"
" s_add_u32 s83,s82,4096\n"
" s_lshr_b32 s84,s69,7\n"
" s_add_u32 s84,s84,s68\n"
" s_mov_b32 s80,0\n"
" s_waitcnt lgkmcnt(0)\n"
" s_sleep " STR(DL1) "\n"
MT(52)
"RL_red:\n"
" buffer_load_dwordx4 v[128:131],v249,%3,s82 offen sc1\n"
" buffer_load_dwordx4 v[132:135],v249,%3,s83 offen sc1\n"
" buffer_load_dwordx4 v[136:139],v249,%3,s82 offen offset:1024 sc1\n"
" buffer_load_dwordx4 v[140:143],v249,%3,s83 offen offset:1024 sc1\n"
" buffer_load_dwordx4 v[144:147],v249,%3,s82 offen offset:2048 sc1\n"
" buffer_load_dwordx4 v[148:151],v249,%3,s83 offen offset:2048 sc1\n"
" buffer_load_dwordx4 v[152:155],v249,%3,s82 offen offset:3072 sc1\n"
" buffer_load_dwordx4 v[156:159],v249,%3,s83 offen offset:3072 sc1\n"
" s_waitcnt vmcnt(7)\n"
" MZ 164,160,128\n"
" s_waitcnt vmcnt(6)\n"
" MA 164,160,132\n"
" s_waitcnt vmcnt(5)\n"
" MA 164,160,136\n"
" s_waitcnt vmcnt(4)\n"
" MA 164,160,140\n"
" s_waitcnt vmcnt(3)\n"
" MA 164,160,144\n"
" s_waitcnt vmcnt(2)\n"
" MA 164,160,148\n"
" s_waitcnt vmcnt(1)\n"
" MA 164,160,152\n"
" s_waitcnt vmcnt(0)\n"
" MA 164,160,156\n"
" s_nop 7\n"
" v_cmp_u_f32 vcc,v164,v164\n"
" v_add_f32_dpp v164,v164,v164 row_ror:8 row_mask:0xf bank_mask:0xf\n"
" v_add_f32_dpp v165,v165,v165 row_ror:8 row_mask:0xf bank_mask:0xf\n"
" v_add_f32_dpp v166,v166,v166 row_ror:8 row_mask:0xf bank_mask:0xf\n"
" v_add_f32_dpp v167,v167,v167 row_ror:8 row_mask:0xf bank_mask:0xf\n"
" s_cbranch_vccnz RL_failr\n"
"RL_okr:\n"
" s_mov_b64 exec,0x00FF00FF\n"
" CV 164,164,165\n"
" CV 165,166,167\n"
" buffer_store_dwordx2 v[164:165],v255,%3,s84 offen sc1\n"
" s_mov_b64 exec,-1\n"
"RL_join:\n"
MT(54)
" s_barrier\n"
" s_add_u32 s78,s78,1\n"
" s_mov_b32 s81,s67\n"
" s_mov_b32 s67,s65\n"
" s_mov_b32 s65,s66\n"
" s_mov_b32 s66,s81\n"
" v_xor_b32 v253,16384,v253\n"
MTACC
" s_sleep " STR(DL2) "\n"
" s_branch RL_loop\n"
"RL_other:\n"
" v_mov_b64 v[164:165],-1\n"
" v_mov_b64 v[166:167],-1\n"
" s_add_u32 s82,s67,s71\n"
" .if (" STR(OPT) "&8)==0\n"
" s_cmp_lt_u32 s85,s86\n"
" s_cbranch_scc1 RL_failf\n"
"RL_okf:\n"
" .endif\n"
" s_xor_b32 s72,s72,128\n"
" s_sleep " STR(DL3) "\n"
" .if (" STR(OPT) "&9)==0\n"
" buffer_load_dwordx4 v249,%3,s70 offen sc1 lds\n"
" buffer_load_dwordx4 v249,%3,s70 offen offset:1024 sc1 lds\n"
" buffer_load_dwordx4 v249,%3,s70 offen offset:2048 sc1 lds\n"
" buffer_load_dwordx4 v249,%3,s70 offen offset:3072 sc1 lds\n"
" .endif\n"
" buffer_load_dword v203,off,%3,s72 sc1\n"
" s_sleep " STR(DL4) "\n"
" .if (" STR(OPT) "&2)==0\n"
" EXPORT\n"
" .endif\n"
" .if (" STR(OPT) "&4)==0\n"
" buffer_store_dwordx4 v[164:167],v249,%3,s82 offen sc1\n"
" buffer_store_dwordx4 v[164:167],v249,%3,s82 offen offset:1024 sc1\n"
" buffer_store_dwordx4 v[164:167],v249,%3,s82 offen offset:2048 sc1\n"
" .endif\n"
" s_add_u32 s70,s70,0x80000\n"
" s_add_u32 s74,s74,s75\n"
" .if " STR(RG) "==7\n"
" s_bitset0_b32 s70,22\n"
" s_lshl_b32 s81,s75,3\n"
" s_andn2_b32 s74,s74,s81\n"
" .endif\n"
" s_xor_b32 s76,s76,16384\n"
" s_branch RL_join\n"
"RL_failm:\n"
" SPIN RL_okm,1\n"
" buffer_load_dwordx4 v[128:131],v254,%3,s68 offen sc1\n"
" buffer_load_dwordx4 v[132:135],v254,%3,s68 offen offset:1024 sc1\n"
" ZR 168,4\n"
" s_waitcnt vmcnt(0)\n"
" s_branch RL_retry\n"
"RL_failr:\n"
" .set zi,129\n"
" .rept 15\n"
" v_max3_u32 v128,v128,v[zi],v[zi+1]\n"
" .set zi,zi+2\n"
" .endr\n"
" v_max_u32 v128,v128,v159\n"
" v_cmp_eq_u32 vcc,-1,v128\n"
" s_cbranch_vccz RL_okr\n"
" SPIN RL_okr,1024\n"
" s_branch RL_red\n"
"RL_failf:\n"
" SPIN RL_okf,0x100000\n"
" buffer_load_dword v200,off,%3,s72 sc1\n"
" s_waitcnt vmcnt(0)\n"
" v_readfirstlane_b32 s85,v200\n"
" s_nop 3\n"
" s_cmp_lt_u32 s85,s86\n"
" s_cbranch_scc1 RL_failf\n"
" s_branch RL_okf\n"
"RL_last:\n"
" s_cmp_eq_u32 s64,0\n"
" s_cbranch_scc1 2f\n"
" s_waitcnt lgkmcnt(0)\n"
" EXPORT\n"
"2:\n"
" v_mov_b32 v202,64\n"
" s_waitcnt vmcnt(0) lgkmcnt(0)\n"
" s_barrier\n"
" buffer_store_dword v202,off,%3,s73 sc1\n"
" s_nop 7\n"
  :"+{s[64:79]}"(sc),"+{v[248:255]}"(vc),"+{s[88:95]}"(pc)
  :"s"(rw),"{a[0:31]}"(wa[0]),"{a[32:63]}"(wa[1]),"{a[64:95]}"(wa[2]),"{a[96:127]}"(wa[3]),"{a[128:159]}"(wa[4]),"{a[160:191]}"(wa[5]),"{a[192:223]}"(wa[6]),"{a[224:255]}"(wa[7]),
   "{v[0:31]}"(wv[0]),"{v[32:63]}"(wv[1]),"{v[64:95]}"(wo[0]),"{v[96:127]}"(wo[1])
  :"memory","scc","vcc",CLOB);
  dead=sc[13];
  TC(21,__builtin_amdgcn_s_memtime()) TS(3) TC(9,sc[15]&1023) TC(10,(sc[15]>>10)&1023)
}
#define SPZ 0
#define DY 3
#define DW 4
#define GL __attribute__((address_space(1)))
#define XB 0
#define DH 8
#define DZ 24
DI u32 mx4(u32 m,u32x4 v){ return max(max(max(m,v.x),v.y),max(v.z,v.w)); }
DI u32x4 pk8(f32x4 a,f32x4 b){ return u32x4{pk(a.x,b.x),pk(a.y,b.y),pk(a.z,b.z),pk(a.w,b.w)}; }
DI u32x4 pkc(f32x4 a,f32x4 b){ return u32x4{pk(a.x,a.y),pk(a.z,a.w),pk(b.x,b.y),pk(b.z,b.w)}; }
DI void roleB1(const Args& A,char* ws,int h,int par,const int w,int& dead){
  const int l=lane(),g=l>>4,n=l&15;
  rsrc_t rw=mk(ws,WS_SZ); const u32 l16=l*16;
  enum{LA=3,L_B=65536,L_M=139264};
  UNR for(int i=0;i<8;i++) dma16(rw,l16,O_XF+par*32768+(w*8+i)*1024,(w*8+i)*1024);
  u32x4 wq[16][2],wk[16][2],wv[16][2];
  { rsrc_t rk=mk(A.wk,4*MB),rv=mk(A.wv,4*MB),rq=mk(A.wq,4*MB); const u32 base=32768+w*28672;
    const u32 va=g*8192+(n>>2)*65536+(n&3)*16,vb=(l>>5)*8192+((l>>3)&3)*65536+(l&7)*16; u32 ra=base+l*4,rb=base+l*8+(w&1)*4; PIN(ra); PIN(rb);
    stream<32,7>(base,[&](int c,u32 d){ u32 so=h*256; asm volatile("":"+s"(so),"+s"(d));
        UNR for(int i=0;i<4;i++){ if(c>=8&&c<24) dmaw(rv,vb,so+(w>>1)*128+((c-8)*32+2*i)*8192,d+i*1024);
          else dmaw(c<8?rk:rq,va,so+w*64+((c&7)*64+(i>>1)*32+(i&1)*4)*8192,d+i*1024); } },
      [&](int c,u32 o){ u32 t[16]; u32x4 f0,f1;
        if(c>=8&&c<24){ UNR for(int i=0;i<8;i++) t[i]=LDS(u32,rb+o+i*512); mkfr(t,f0,f1);
          if(c<17){ f0=toA(f0); f1=toA(f1); } else { PIN(f0); PIN(f1); } wv[c-8][0]=f0; wv[c-8][1]=f1; }
        else { UNR for(int i=0;i<16;i++) t[i]=LDS(u32,ra+o+i*256);
          UNR for(int q=0;q<2;q++){ mkfr(t+8*q,f0,f1); if(c<8){ wk[2*c+q][0]=toA(f0); wk[2*c+q][1]=toA(f1); } else { PIN(f0); PIN(f1); wq[2*(c&7)+q][0]=f0; wq[2*(c&7)+q][1]=f1; } } } }); }
  int bi=0,cf=0,cg=0; const u32 fo=O_FL+F_O+((par*2+(w&1))*32+h)*128+(w>>1)*4; vm0(); TS(1)
  for(int i=0;i<32+LA;i++){
    const int s=2*i+par,sa=s-2*LA; const u32 lb=L_B+bi*24576+l16; u32x4 pq[2],pk_[2],pv[2];
    bool pub=i>LA; u32 fl=pub?LDS(u32,151552+w*1024+l16):0; bar();
    if(i>=LA){
      if(__any(fl<(u32)sa+1)){ if(pub){ vm0(); st1c(rw,0,fo,sa-1); pub=false; } waitf(rw,O_FL+F_KM+2*h*128,2,sa+1,dead,128); CNT(cf,1) }
      if(i==32) TS(2) if(i==34) TS(3)
    }
#define KMD(j) { if(j==0) dma16(rw,(2*(n>>2)+(n&1))*256+g*16,O_KM+((sa&RG)*32+h)*2048+w*64,L_M+w*1024); \
      else dma16(rw,l16,O_VM+((sa&RG)*32+h)*8192+(2*w+j-1)*1024,L_M+4096+(2*w+j-1)*1024); }
    if(i<32){
      f32x4 cq[2][2]={},ck[2][2]={},cv[2][2]={}; u32 xs=O_XF+(s+2)*32768+w*8192,xd=((i+1)&1)*32768+w*8192;
      u32x4 xx[2][2]; const u32 xb=(i&1)*32768+l16; xx[0][0]=LDS(u32x4,xb); xx[0][1]=LDS(u32x4,xb+16384);
      UNR for(int ks=0;ks<16;ks++){
        UNR for(int kt=0;kt<2;kt++){ u32x4 x=xx[ks&1][kt];
          ck[kt][0]=mfma(wk[ks][0],x,ck[kt][0]); SB(); xx[(ks+1)&1][kt]=LDS(u32x4,xb+(((ks+1)&15)+16*kt)*1024); SB();
          cv[kt][0]=mfma(x,wv[ks][0],cv[kt][0]); ck[kt][1]=mfma(wk[ks][1],x,ck[kt][1]); cv[kt][1]=mfma(x,wv[ks][1],cv[kt][1]); SB(); }
        if(ks<3) KMD(ks) else if(ks<11){ dma16(rw,l16,xs,xd); xs+=1024; xd+=1024; asm volatile("":"+s"(xs),"+s"(xd)); }
        SB(); }
      const u32 gb=O_KV+((s&RG)*32+h)*16384;
      UNR for(int ks=0;ks<16;ks++){ const int j=ks>>3,q=ks&3;
        UNR for(int kt=0;kt<2;kt++){ u32x4 x=xx[ks&1][kt];
          cq[kt][0]=mfma(wq[ks][0],x,cq[kt][0]); SB(); if(ks<15) xx[(ks+1)&1][kt]=LDS(u32x4,xb+(ks+1+16*kt)*1024); SB();
          cq[kt][1]=mfma(wq[ks][1],x,cq[kt][1]); SB();
          if(kt==0){ if(ks&4) pv[j][q]=pk(cv[q>>1][j][(q&1)*2],cv[q>>1][j][(q&1)*2+1]); else pk_[j][q]=pk(ck[j][0][q],ck[j][1][q]); SB(); } }
        if(ks==3){ vm0(); if(pub) st1c(rw,0,fo,sa-1); pub=false; }
        if(q==3){ if(ks&4) st4c(rw,l16,gb+8192+(2*w+j)*1024,pv[j]); else st4c(rw,l16,gb+(j*4+w)*1024,pk_[j]); }
        SB(); }
      UNR for(int j=0;j<2;j++) pq[j]=toA(pk8(cq[j][0],cq[j][1]));

    } else { KMD(0) KMD(1) KMD(2) }
    if(i>=LA){
      const int qt=w&1,eh=w>>1; f32x4 s0={},s1={},sm={}; u32x4 qf[4],kf[4];
      UNR for(int k=0;k<4;k++){ qf[k]=LDS(u32x4,lb+16384+(qt*4+k)*1024); kf[k]=LDS(u32x4,lb+k*1024); } SB();
      UNR for(int k=0;k<4;k++){ s0=mfma(kf[k],qf[k],s0); SB(); kf[k]=LDS(u32x4,lb+(4+k)*1024); SB(); }
      UNR for(int k=0;k<4;k++) s1=mfma(kf[k],qf[k],s1);
      SB(); if(i>=32) vm0();
      if(pub) st1c(rw,0,fo,sa-1);
      dma16(rw,(l16&16)*8,O_FL+F_KM+2*h*128,151552+w*1024); bar();
      UNR for(int k=0;k<4;k++) kf[k]=LDS(u32x4,L_M+k*1024+l16); SB();
      UNR for(int k=0;k<4;k++){ sm=mfma(kf[k],qf[k],sm); SB(); kf[k]=LDS(u32x4,lb+8192+(eh*4+k)*1024); qf[k]=LDS(u32x4,L_M+4096+(eh*4+k)*1024+l16); SB(); }
      const int dq=8*(n>>2)+4*qt+(n&3)-8*g; float pe[10],mx=fmaxf(sm.x,sm.y)*CEXP;
      UNR for(int j=0;j<8;j++){ pe[j]=j<=dq?(j<4?s0[j&3]:s1[j&3])*CEXP:-1.4426950e9f; mx=fmaxf(mx,pe[j]); }
      pe[8]=sm.x*CEXP; pe[9]=sm.y*CEXP; mx=gmax(mx); float su=0;
      UNR for(int j=0;j<10;j++){ pe[j]=__builtin_amdgcn_exp2f(pe[j]-mx); su+=pe[j]; }
      su=__builtin_amdgcn_rcpf(gsum(su));
      u32x4 pf={pk(pe[0],pe[1]),pk(pe[2],pe[3]),pk(pe[4],pe[5]),pk(pe[6],pe[7])}; u32 p1=pk(pe[8],pe[9]); u32x4 pm={p1,p1,p1,p1};
      f32x4 d[4];
      UNR for(int t=0;t<4;t++){ d[t]=mfma(kf[t],pf,f32x4{0,0,0,0});
        d[t]=mfma(u32x4{qf[(t&1)*2][t&2],qf[(t&1)*2][(t&2)+1],qf[(t&1)*2+1][t&2],qf[(t&1)*2+1][(t&2)+1]},pm,d[t])*su; }
      SB(); vm0();
      UNR for(int j=0;j<2;j++) st4c(rw,l16,O_OF+((2*sa+qt)*128+h*4+eh*2+j)*1024,
        u32x4{pk(d[0][2*j],d[1][2*j]),pk(d[2][2*j],d[3][2*j]),pk(d[0][2*j+1],d[1][2*j+1]),pk(d[2][2*j+1],d[3][2*j+1])});
    } else vm0();
    if(i<32){ bar();
      if(w==0) st1c(rw,0,O_FL+F_KV+(2*h+par)*128,s+1);
      UNR for(int j=0;j<2;j++){ LDS(u32x4,lb+(j*4+w)*1024)=pk_[j]; LDS(u32x4,lb+8192+(2*w+j)*1024)=pv[j]; LDS(u32x4,lb+16384+(j*4+w)*1024)=pq[j]; } }
    bi=bi==LA-1?0:bi+1;
  }
  vm0(); st1c(rw,0,fo,64); bar();
  TC(9,cf) TC(10,cg)
}
DI float gelu(float x){ float u=x*(1.f+0.044715f*x*x)*-2.3022082f; return x*__builtin_amdgcn_rcpf(1.f+__builtin_amdgcn_exp2f(u)); }
DI void roleB2(const Args& A,char* ws,int grp,int j,const int w,int& dead){
  const int l=lane(),g=l>>4,n=l&15;
  rsrc_t rw=mk(ws,WS_SZ); const u32 l16=l*16;
  waitf(rw,O_FL+F_KV+(grp*16+j)*128,1,1,dead);
  u32x4 wo[32][2],wi[16][2];
  const int un=j+16*w,isz2=un>=22,u=un-22*isz2;
  { rsrc_t ro=mk(A.wo,4*MB),ri=mk(A.win,512*1376*2); const u32 base=w*32768,va=g*1024+(n>>2)*16384+(n&3)*16,vb=(g+(n>>2)*8)*2752+(n&(u==21?1:3))*16,
      mk_=(u==21&&n>=8)?0:~0u; u32 ra=base+l*4; PIN(ra);
    stream<24,8>(base,[&](int c,u32 d){ u32 so=c<16?j*64+w*MB:un<44?isz2*1376+u*64:0; asm volatile("":"+s"(so),"+s"(d));
        UNR for(int i=0;i<4;i++){ if(c<16) dmaw(ro,va,so+(c*64+i*4)*1024,d+i*1024); else dmaw(ri,vb,so+((c&7)*64+(i>>1)*32+(i&1)*4)*2752,d+i*1024); } },
      [&](int c,u32 o){ u32 t[16]; u32x4 f0,f1; UNR for(int i=0;i<16;i++){ t[i]=LDS(u32,ra+o+i*256); if(c>=16) t[i]&=mk_; }
        UNR for(int q=0;q<2;q++){ mkfr(t+8*q,f0,f1); if(c<15){ f0=toA(f0); f1=toA(f1); } else { PIN(f0); PIN(f1); }
          if(c<16){ wo[2*c+q][0]=f0; wo[2*c+q][1]=f1; } else { wi[2*(c&7)+q][0]=f0; wi[2*(c&7)+q][1]=f1; } } }); }
  { const u32x4 sv={SENT,SENT,SENT,SENT}; const int c=(grp*16+j)*4+w;
    UNR for(int i=0;i<8;i++) st4c(rw,l16+(i&3)*1024,O_HF+(c*8+(i&4))*1024,sv);
    UNR for(int i=0;i<11;i++){ st4c(rw,l16+(i&3)*1024,O_Z1+(c*11+(i&12))*1024,sv); st4c(rw,l16+(i&3)*1024,O_Z2+(c*11+(i&12))*1024,sv); st4c(rw,l16+(i&3)*1024,O_UF+(c*11+(i&12))*1024,sv); }
    UNR for(int i=0;i<2;i++) st4c(rw,l16,O_ST+(c*2+i)*1024,sv);
    vm0(); bar(); if(w==0) st1c(rw,0,O_FL+F_FI+c*32,1); }
  waitf(rw,O_FL+F_FI,64,1,dead,128);
  TS(1)
  enum{L_X=131072,L_P=147456-2048,L_S=153600};
  const u32x4 one={0x3F803F80u,0x3F803F80u,0x3F803F80u,0x3F803F80u}; f32x4 cs0={},cs1={};
  if(!isz2){ UNR for(int k=0;k<16;k++){ cs0=mfma(wi[k][0],one,cs0); cs1=mfma(wi[k][1],one,cs1); } }
  else     { UNR for(int k=0;k<16;k++){ cs0=mfma(one,wi[k][0],cs0); cs1=mfma(one,wi[k][1],cs1); } }
  u32 lx=L_X+l16,lo=w*32768+l16; PIN(lx); PIN(lo); PIN(cs0); PIN(cs1);
  int cf=0,cg=0; rsrc_t rx=mk(A.x,2*MB); const u32 fv=(l&7)*128+(l&8)/2;
  PT0
  for(int it=0;;it++){
    const int blk=grp+4*it,pb=blk-4; const u32 fo=O_FL+F_O+(grp*32+w*8)*128,need=(blk>>1)+1,ob=O_OF+(blk*128+w*32)*1024;
    bool rdy=false;
    if(it){ const int s=pb>>1,kt=pb&1; int sp=0;
      if(w) __builtin_amdgcn_s_sleep(DH+5); else __builtin_amdgcn_s_sleep(DH);
      PT(0) u32x4 x[16]; u32 fl;
      for(;;){ asm volatile("":::"memory"); UNR for(int i=0;i<4;i++) x[i]=ld4c(rw,l16+i*1024,O_HF+(pb*16+w*4)*1024);
        fl=ld1c(rw,fv,fo); u32 mx=0; UNR for(int i=0;i<4;i++) mx=mx4(mx,x[i]);
        if(dead||!__any(mx==SENT)) break; if(++sp>SPINCAP){dead=1;break;} }
      PT(1) UNR for(int i=0;i<4;i++) LDS(u32x4,lx+(w*4+i)*1024)=x[i];
      rdy=!__any(fl<need); bar();
      UNR for(int k=0;k<16;k++) x[k]=LDS(u32x4,lx+k*1024); SB();
      if(it==32) TS(3)
      f32x4 z0={},z1={};
#define OD if((k&3)==0){ SB(); if(rdy) dmaN<1>(rw,l16,ob+k*1024,w*32768+k*1024); SB(); }
      if(w==3){ UNR for(int k=0;k<16;k++){ z0=mfma(x[k],x[k],z0); z1=mfma(one,x[k],z1); OD }
        float mu=z1.x*(1.f/512),q=(n&2)?((n&1)?z0.w:z0.z):((n&1)?z0.y:z0.x);
        if(g==n>>2){ LDS(float,L_S+n*4)=mu; LDS(float,L_S+64+n*4)=rsqrtf(fmaxf(q*(1.f/512)-mu*mu,0.f)+1e-5f); } }
      else if(!isz2){ UNR for(int k=0;k<16;k++){ z0=mfma(wi[k][0],x[k],z0); z1=mfma(wi[k][1],x[k],z1); OD } }
      else     { UNR for(int k=0;k<16;k++){ z0=mfma(x[k],wi[k][0],z0); z1=mfma(x[k],wi[k][1],z1); OD } }
      bar();
      if(un<44){ f32x4 mu,rs;
        if(isz2){ mu=LDS(f32x4,L_S+g*16); rs=LDS(f32x4,L_S+64+g*16); } else { float a=LDS(float,L_S+n*4),b=LDS(float,L_S+64+n*4); mu=f32x4{a,a,a,a}; rs=f32x4{b,b,b,b}; }
        UNR for(int i=0;i<4;i++){ z0[i]=(z0[i]-mu[i]*cs0[i])*rs[i]; z1[i]=(z1[i]-mu[i]*cs1[i])*rs[i]; }
        UNR for(int i=0;i<4;i++){ z0[i]=gelu(z0[i]); z1[i]=gelu(z1[i]); }
        if(!isz2) st4c(rw,l16,O_Z1+(pb*22+u)*1024,pk8(z0,z1));
        else { u32x4 v=pkc(z0,z1); st4c(rw,l16,O_Z2+(s*44+2*u+kt)*1024,v);
          f32x4 a,b; UNR for(int i=0;i<4;i++){ float p=(i&1)?bhi(v[i>>1]):blo(v[i>>1]),q=(i&1)?bhi(v[2+(i>>1)]):blo(v[2+(i>>1)]); a[i]=rsum16(p+q); b[i]=rsum16(p*p+q*q); }
          if(n<2) st4c(rw,g*32+n*16,O_ST+(s*22+u)*256+kt*128,__builtin_bit_cast(u32x4,n?f32x4{a.z,a.w,b.z,b.w}:f32x4{a.x,a.y,b.x,b.y})); } }
      if(it==32){ TC(9,cf) TC(10,cg) PTE break; }
      PT(2)
    }
    if(w==0) dmaw(rx,(8*(n>>2)+(n&3))*1024+g*16,((blk>>1)*32+4*(blk&1))*1024+j*64,154624);
    if(!rdy){ waitf(rw,fo,64,need,dead,0,fv); CNT(cf,1) if(it==31) TS(2) dmaN<4>(rw,l16,ob,w*32768); }
    PT(3) f32x4 c0={},c1={}; u32x4 o[2][4];
    UNR for(int b=0;b<=8;b++){
      if(b<8) asm volatile("s_waitcnt vmcnt(%0)"::"n"(b<4?12:28-4*b):"memory");
      UNR for(int i=0;i<4;i++){ if(b<8) o[b&1][i]=LDS(u32x4,lo+(b*4+i)*1024); SB();
        if(b>0){ c0=mfma(wo[(b-1)*4+i][0],o[(b-1)&1][i],c0); c1=mfma(wo[(b-1)*4+i][1],o[(b-1)&1][i],c1); } SB(); }
      if(b<4) dmaN<1>(rw,l16,ob+16384+b*4096,w*32768+16384+b*4096);
      SB(); }
    PT(4)
    if(w){ LDS(f32x4,L_P+w*2048+l16)=c0; LDS(f32x4,L_P+w*2048+1024+l16)=c1; }
    bar();
    if(w==0){ UNR for(int q=1;q<4;q++){ c0+=LDS(f32x4,L_P+q*2048+l16); c1+=LDS(f32x4,L_P+q*2048+1024+l16); }
      u32x4 a=pk8(c0,c1),hh,xr=LDS(u32x4,154624+l16); UNR for(int i=0;i<4;i++) hh[i]=pk(blo(a[i])+blo(xr[i]),bhi(a[i])+bhi(xr[i]));
      st4c(rw,l16,O_HF+(blk*16+j)*1024,hh); }
    PT(5)
  }
}
DI void loadF(const Args& A,u32x4 (&wf)[22][2],int nq,const int w){
  const int l=lane(),g=l>>4,n=l&15; rsrc_t ro=mk(A.wout,688*1024);
  const u32 base=w*32768,va=g*1024+(n>>2)*8192+(n&3)*16; u32 ra=base+l*4; PIN(ra); bar();
  stream<11,8>(base,[&](int c,u32 d){ u32 so=nq*256+w*64; asm volatile("":"+s"(so),"+s"(d));
      UNR for(int i=0;i<4;i++) dmaw(ro,(c==10&&i>=2)?va&~16384u:va,so+(c*64+(i>>1)*32+(i&1)*4)*1024,d+i*1024); },
    [&](int c,u32 o){ u32 t[16]; u32x4 f0,f1; UNR for(int i=0;i<16;i++) t[i]=LDS(u32,ra+o+i*256);
      UNR for(int q=0;q<2;q++){ mkfr(t+8*q,f0,f1); if(c<9){ f0=toA(f0); f1=toA(f1); } else { PIN(f0); PIN(f1); } wf[2*c+q][0]=f0; wf[2*c+q][1]=f1; } });
  bar();
}
template<int OWN> DI void roleF(const Args& A,char* ws,u32x4 (&wf)[22][2],int tb,int nq,const int w,int& dead){
  const int l=lane(),g=l>>4,n=l&15;
  rsrc_t rw=mk(ws,WS_SZ); const u32 l16=l*16,ub=O_UF+(tb*44+w*11)*1024; int sp=0;
  if(!OWN){ waitf(rw,O_FL+F_FI,64,1,dead,128); TS(5)
    while(!dead&&__any(ld1c(rw,min(n,10)*1024,ub)==SENT)){ if(++sp>SPINCAP){dead=1;break;} asm volatile("s_sleep 2":::"memory"); }
    TS(6) }
  u32x4 hf[2],uf[11];
  for(;;){ asm volatile("":::"memory"); u32 mx=0;
    if(!OWN){ UNR for(int i=0;i<11;i++) uf[i]=ld4c(rw,l16+(i&3)*1024,ub+(i&12)*1024); }
    UNR for(int i=0;i<2;i++) hf[i]=ld4c(rw,l16,O_HF+((tb*2+i)*16+4*nq+w)*1024);
    if(!OWN){ UNR for(int i=0;i<11;i++){ mx=mx4(mx,uf[i]); LDS(u32x4,(w*11+i)*1024+l16)=uf[i]; } }
    mx=mx4(mx4(mx,hf[0]),hf[1]);
    if(dead||!__any(mx==SENT)) break; if(++sp>SPINCAP){dead=1;break;} }
  bar();
  f32x4 c[2][2]={}; u32x4 x[6]; UNR for(int i=0;i<5;i++) x[i]=LDS(u32x4,((i&1)*22+(i>>1))*1024+l16);
  UNR for(int i=0;i<44;i++){ c[i&1][0]=mfma(wf[i>>1][0],x[i%6],c[i&1][0]); SB();
    if(i+5<44) x[(i+5)%6]=LDS(u32x4,(((i+5)&1)*22+((i+5)>>1))*1024+l16); SB();
    c[i&1][1]=mfma(wf[i>>1][1],x[i%6],c[i&1][1]); SB(); }
  UNR for(int kt=0;kt<2;kt++){ u32x4 a=pk8(c[kt][0],c[kt][1]),v; UNR for(int i=0;i<4;i++) v[i]=pk(blo(a[i])+blo(hf[kt][i]),bhi(a[i])+bhi(hf[kt][i]));
    ((GL u32x4*)A.out)[(tb*32+8*(n>>2)+4*kt+(n&3))*64+nq*16+w*4+g]=v; }
}
DI void roleB3(const Args& A,char* ws,int tb,const int w,int& dead){
  const int l=lane(),g=l>>4,n=l&15;
  rsrc_t rw=mk(ws,WS_SZ),rs_=mk(A.wsp,8*MB); const u32 l16=l*16;
  f32x4 c[12][2]={}; float cr[2]={0,0}; u32x4 z[12],y[12]={}; int cf=0;
  const int tk=tb*32+8*(n>>2)+(n&3); const int nt=w<2?12:10,t0=w<2?12*w:10*w+4;
  { u32 so=w*MB+tb*16384+__builtin_amdgcn_readfirstlane(ld1(rw,0,O_FL+F_SEL+(w>>1)*4));
    if(w<3||tb){ UNR for(int i=0;i<8;i++) st4c(rw,l16+(i&3)*1024,so+(i&4)*1024,u32x4{SENT,SENT,SENT,SENT}); }
    vm0(); bar(); if(w==0) st1c(rw,0,O_FL+F_RI+tb*128,1); }
  UNR for(int i=0;i<DW;i++) __builtin_amdgcn_s_sleep(37);
  u32x4 wf[22][2]; loadF(A,wf,3,w);
  for(int i=0;i<32;i++) dma16(rs_,tk*4096+g*16,(w*16+(i>>2)*2+(i&1))*64+(i&2)*8192,((w*16+(i>>2)*2+(i&1))*2+((i>>1)&1))*1024);
  vm0(); TS(1) waitf(rw,O_FL+F_O+tb*128,1,7,dead); waitf(rw,O_FL+F_FI,64,1,dead,128); bar();
  float bsp[2]; UNR for(int kt=0;kt<2;kt++){ bsp[kt]=blo(((const GL unsigned short*)A.bsp)[tk+4*kt]); PIN(bsp[kt]); }
  const u32 v0=n*256+g*32,v1=n<6?v0+4096:0x7FFFF000u,sb=O_ST+(w>>1)*128+(w&1)*16,lw=l16+w*4; u32x4 ta={SENT},tc={};
  for(int s=0;s<64;s++){
    int sp=0; const bool ly=s==max(tb,61);
#define LDZ { UNR for(int i=0;i<12;i++) if(i<nt) z[i]=ld4c(rw,l16+(i&3)*1024,O_Z2+(s*44+t0+(i&12))*1024); \
      if(ly){ UNR for(int i=0;i<12;i++) if(i<nt) y[i]=ld4c(rw,l16,O_Z1+((tb*2+(i&1))*22+(t0>>1)+(i>>1))*1024); } }
    while(!dead&&__any(mx4(mx4(0,ta),tc)==SENT)){ if(++sp>SPINCAP){dead=1;break;} if(s<61) __builtin_amdgcn_s_sleep(DZ); else if(sp>1) __builtin_amdgcn_s_sleep(DY); ta=ld4c(rw,v0,sb+s*5632); tc=ld4c(rw,v1,sb+s*5632); if(SPZ&&s==63) LDZ }
    f32x4 st=__builtin_bit_cast(f32x4,ta)+__builtin_bit_cast(f32x4,tc); u32 d0=LDS(u32,s*2048+lw),d1=LDS(u32,s*2048+1024+lw); if(s==63) TS(2)
    SB(); if(!(SPZ&&s==63&&sp)) LDZ ta=ld4c(rw,v0,sb+s*5632+5632); tc=ld4c(rw,v1,sb+s*5632+5632); SB();
    UNR for(int i=0;i<4;i++) st[i]=rsum16(st[i])*(1.f/688);
    float ra=rsqrtf(fmaxf(st.z-st.x*st.x,0.f)+1e-5f),rb=rsqrtf(fmaxf(st.w-st.y*st.y,0.f)+1e-5f);
    d0=pk(blo(d0)*ra,bhi(d0)*rb); d1=pk(blo(d1)*ra,bhi(d1)*rb); LDS(u32,s*2048+lw)=d0; LDS(u32,s*2048+1024+lw)=d1;
    cr[0]+=blo(d0)*st.x+bhi(d0)*st.y; cr[1]+=blo(d1)*st.x+bhi(d1)*st.y;
    if(s==63){ UNR for(int kt=0;kt<2;kt++){ float v=gsum(cr[kt]); if(g==0) LDS(float,131072+kt*256+lw)=v; } }
    bar(); const u32x4 b0=LDS(u32x4,s*2048+l16),b1=LDS(u32x4,s*2048+1024+l16); SB();
    for(;;){ u32 mx=0; UNR for(int i=0;i<12;i++) if(i<nt) mx=mx4(mx,z[i]);
      if(ly){ UNR for(int i=0;i<12;i++) if(i<nt) mx=mx4(mx,y[i]); }
      if(dead||!__any(mx==SENT)) break; if(++sp>SPINCAP){dead=1;break;} CNT(cf,1) asm volatile("":::"memory"); LDZ }
    UNR for(int i=0;i<12;i++) if(i<nt){ const int o=(i&1)*2; u32x4 a={z[i&14][o],z[i&14][o+1],z[i|1][o],z[i|1][o+1]}; c[i][0]=mfma(a,b0,c[i][0]); c[i][1]=mfma(a,b1,c[i][1]); }
  }
  TS(3) TC(9,cf)
  UNR for(int kt=0;kt<2;kt++){ f32x4 cc=LDS(f32x4,131072+kt*256+n*16); float bs=bsp[kt]-(cc.x+cc.y+cc.z+cc.w);
    UNR for(int i=0;i<6;i++) if(2*i<nt){ u32x4 v; UNR for(int q=0;q<4;q++) v[q]=pk(blo(y[2*i+kt][q])*(c[2*i][kt][q]+bs),bhi(y[2*i+kt][q])*(c[2*i+1][kt][q]+bs));
      st4c(rw,l16,O_UF+((tb*2+kt)*22+(t0>>1)+i)*1024,v); LDS(u32x4,(kt*22+(t0>>1)+i)*1024+l16)=v; } }
  TS(5) roleF<1>(A,ws,wf,tb,3,w,dead);
}
DI Args AR(){ typedef const __attribute__((address_space(4))) u64* AP; AP p=(AP)__builtin_amdgcn_kernarg_segment_ptr(); asm volatile("":"+s"(p));
  return Args{(void*)p[0],(void*)p[1],(void*)p[2],(void*)p[3],(void*)p[4],(void*)p[5],(void*)p[6],(void*)p[7],(void*)p[8],(void*)p[9],(void*)p[10],(void*)p[11]}; }
__global__ void __launch_bounds__(256,1) main_kernel(Args A){
  __shared__ __attribute__((aligned(1024))) char smem[LDS_SZ];
  asm volatile(""::"v"(smem):"memory");
  char* ws=wsbase(A.ws); const int b=blockIdx.x,x=b&7,y=b>>3; int dead=0; const int w=__builtin_amdgcn_readfirstlane(threadIdx.x>>6);
  TS(0)
  if(y>=16&&x<4) roleB3(AR(),ws,x*16+y-16,w,dead);
  else {
    if(x<4) roleR(AR(),ws,8*x+(y>>1),y&1,w,dead); else if(y<16) roleB1(AR(),ws,8*(x-4)+(y>>1),y&1,w,dead); else roleB2(AR(),ws,x-4,y-16,w,dead);
    TS(4) u32x4 wf[22][2]; const Args B=AR(); const int nq=(x>>2)+(y>>4); loadF(B,wf,nq,w); roleF<0>(B,ws,wf,(x&3)*16+(y&15),nq,w,dead);
  }
  TS(7)
}
extern "C" void kernel_launch(void* const* d_in,const int* in_sizes,int n_in,void* d_out,int out_size,void* d_ws,size_t ws_size,hipStream_t stream){
  Args A{d_in[0],d_in[1],d_in[2],d_in[3],d_in[4],d_in[5],d_in[8],d_in[9],d_in[10],d_in[11],d_out,d_ws};
  pre_kernel<<<258,256,0,stream>>>(A);
  main_kernel<<<256,256,0,stream>>>(A);
}
```

```cpp
#include <hip/hip_runtime.h>
#include <hip/hip_bf16.h>
typedef unsigned u32;
typedef unsigned long long u64;
typedef __attribute__((ext_vector_type(2))) u32 u32x2;
typedef __attribute__((ext_vector_type(4))) u32 u32x4;
typedef __attribute__((ext_vector_type(8))) u32 u32x8;
typedef __attribute__((ext_vector_type(2))) float f32x2;
typedef __attribute__((ext_vector_type(4))) float f32x4;
typedef __attribute__((ext_vector_type(2))) __bf16 bfx2;
typedef __attribute__((ext_vector_type(8))) __bf16 bfx8;
typedef __amdgpu_buffer_rsrc_t rsrc_t;
#define DI __device__ __forceinline__
#define UNR _Pragma("unroll")
#define SB() __builtin_amdgcn_sched_barrier(0)
#define SENT 0xFFFFFFFFu
#define SPINCAP 400000
#define MB (1u<<20)
#define O_M   (3*MB)
#define O_FL  (4*MB)
#define F_SEL 0
#define F_KV  1024
#define F_KM  (F_KV+8192)
#define F_O   (F_KM+8192)
#define F_RI  (F_O+16384)
#define F_FI  (F_RI+8192)
#define FL_SZ 65536
#define O_XF  (5*MB)
#define O_HF  (7*MB)
#define O_Z1  (9*MB)
#define O_Z2  (12*MB)
#define O_UF  (15*MB)
#define O_ST  (18*MB)
#define O_KM  (20*MB)
#define O_KV  (24*MB)
#define O_VM  (56*MB)
#define O_OF  (72*MB)
#define WS_SZ (88*MB)
#define LDS_SZ (152*1024)

DI rsrc_t mk(const void* p,u32 n){ return __builtin_amdgcn_make_buffer_rsrc((void*)p,0,n,0x00020000); }
DI u32x4 ld4(rsrc_t r,u32 v,u32 s){ return __builtin_amdgcn_raw_buffer_load_b128(r,v,s,0); }
DI u32x2 ld2(rsrc_t r,u32 v,u32 s){ return __builtin_amdgcn_raw_buffer_load_b64(r,v,s,0); }
DI u32   ld1(rsrc_t r,u32 v,u32 s){ return __builtin_amdgcn_raw_buffer_load_b32(r,v,s,0); }
DI u32x4 ld4c(rsrc_t r,u32 v,u32 s){ return __builtin_amdgcn_raw_buffer_load_b128(r,v,s,16); }
DI u32x2 ld2c(rsrc_t r,u32 v,u32 s){ return __builtin_amdgcn_raw_buffer_load_b64(r,v,s,16); }
DI u32   ld1c(rsrc_t r,u32 v,u32 s){ return __builtin_amdgcn_raw_buffer_load_b32(r,v,s,16); }
DI void st4c(rsrc_t r,u32 v,u32 s,u32x4 d){ __builtin_amdgcn_raw_buffer_store_b128(d,r,v,s,16); }
DI void st2c(rsrc_t r,u32 v,u32 s,u32x2 d){ __builtin_amdgcn_raw_buffer_store_b64(d,r,v,s,16); }
DI void st1c(rsrc_t r,u32 v,u32 s,u32 d){ __builtin_amdgcn_raw_buffer_store_b32(d,r,v,s,16); }
DI void dma16(rsrc_t r,u32 v,u32 s,u32 lds){ asm volatile("s_mov_b32 m0, %3\n s_nop 0\n buffer_load_dwordx4 %0, %1, %2 offen sc1 lds"::"v"(v),"s"(r),"s"(s),"s"(lds):"memory"); }
#define SCX "sc1"
template<int N4> DI void dmaN(rsrc_t r,u32 v,u32 s,u32 lds){ asm volatile("s_mov_b32 m0, %3\n .rept %4\n s_nop 0\n .irp o,0,1024,2048,3072\n buffer_load_dwordx4 %1, %2, %0 offen offset:\\o " SCX " lds\n .endr\n s_add_u32 m0,m0,4096\n s_add_u32 %0,%0,4096\n .endr":"+s"(s):"v"(v),"s"(r),"s"(lds),"n"(N4):"memory","scc"); }
DI void vm0(){ asm volatile("s_waitcnt vmcnt(0)":::"memory"); }
DI void bar(){ asm volatile("s_waitcnt lgkmcnt(0)\n s_barrier":::"memory"); }
DI u32 pk(float a,float b){ f32x2 v={a,b}; return __builtin_bit_cast(u32,__builtin_convertvector(v,bfx2)); }
DI float blo(u32 v){ return __uint_as_float(v<<16); }
DI float bhi(u32 v){ return __uint_as_float(v&0xFFFF0000u); }
DI u32 plo(u32 a,u32 b){ return __builtin_amdgcn_perm(b,a,0x05040100u); }
DI u32 phi(u32 a,u32 b){ return __builtin_amdgcn_perm(b,a,0x07060302u); }
DI f32x4 mfma(u32x4 a,u32x4 b,f32x4 c){ return __builtin_amdgcn_mfma_f32_16x16x32_bf16(__builtin_bit_cast(bfx8,a),__builtin_bit_cast(bfx8,b),c,0,0,0); }
DI float gmax(float x){ u32 u=__float_as_uint(x); auto a=__builtin_amdgcn_permlane16_swap(u,u,false,false); x=fmaxf(__uint_as_float(a[0]),__uint_as_float(a[1]));
  u=__float_as_uint(x); auto b=__builtin_amdgcn_permlane32_swap(u,u,false,false); return fmaxf(__uint_as_float(b[0]),__uint_as_float(b[1])); }
DI float gsum(float x){ u32 u=__float_as_uint(x); auto a=__builtin_amdgcn_permlane16_swap(u,u,false,false); x=__uint_as_float(a[0])+__uint_as_float(a[1]);
  u=__float_as_uint(x); auto b=__builtin_amdgcn_permlane32_swap(u,u,false,false); return __uint_as_float(b[0])+__uint_as_float(b[1]); }
template<int C> DI float dppf(float x){ return __int_as_float(__builtin_amdgcn_update_dpp(0,__float_as_int(x),C,0xF,0xF,true)); }
DI float rsum16(float x){ x+=dppf<0x128>(x); x+=dppf<0x124>(x); x+=dppf<0x122>(x); x+=dppf<0x121>(x); return x; }
template<class T> DI T toA(T v){ T o; asm("":"=a"(o):"0"(v)); return o; }
#define LDS(T,off) (*(__attribute__((address_space(3))) T*)(size_t)(off))
DI int lane(){ int l; asm volatile("v_mbcnt_lo_u32_b32 %0, -1, 0\n v_mbcnt_hi_u32_b32 %0, -1, %0":"=v"(l)); __builtin_assume(l>=0&&l<64); return l; }
DI void waitf(rsrc_t r,u32 off,u32 n,u32 need,int& dead,u32 str=4,u32 vo=0){
  if(n>1&&str) vo=(lane()&(n-1))*str; int sp=0;
  while(!dead){ u32 v=ld1c(r,vo,off); if(!__any(v<need)) break; if(++sp>SPINCAP){dead=1;break;} __builtin_amdgcn_s_sleep(2); }
  asm volatile("":::"memory");
}
DI void mkfr(const u32* t,u32x4& f0,u32x4& f1){
  f0=u32x4{plo(t[0],t[1]),plo(t[2],t[3]),plo(t[4],t[5]),plo(t[6],t[7])};
  f1=u32x4{phi(t[0],t[1]),phi(t[2],t[3]),phi(t[4],t[5]),phi(t[6],t[7])};
}
DI void dmaw(rsrc_t r,u32 v,u32 s,u32 lds){ asm volatile("s_mov_b32 m0, %3\n s_nop 0\n buffer_load_dwordx4 %0, %1, %2 offen lds"::"v"(v),"s"(r),"s"(s),"s"(lds):"memory"); }
template<int NC,int NR,class I,class U> DI void stream(u32 base,I issue,U use){
  UNR for(int c=0;c<NR;c++) issue(c,base+c*4096);
  UNR for(int c=0;c<NC;c++){ asm volatile("s_waitcnt vmcnt(%0)"::"n"(4*((NC-c<NR?NC-c:NR)-1)):"memory");
    use(c,(c%NR)*4096); SB(); asm volatile("s_waitcnt lgkmcnt(0)":::"memory");
    if(c+NR<NC) issue(c+NR,base+(c%NR)*4096); SB(); }
}
#define TC(i,v)
#define CNT(c,v)
#define PT(i)
#define PT0
#define PTE
#define TS(i) {}
#undef PT
#undef PT0
#undef PTE
#define PT(i)
#define PT0
#define PTE
struct Args{ const void *x,*mem0,*wq,*wk,*wv,*wo,*win,*wsp,*bsp,*wout; void *out,*ws; };
DI char* wsbase(void* ws){ return (char*)(((size_t)ws+(2*MB-1))&~(size_t)(2*MB-1)); }

#define NCAL 4
#define PSC 0
__global__ void __launch_bounds__(256) pre_kernel(Args A){
  char* ws=wsbase(A.ws); rsrc_t rw=mk(ws,WS_SZ);
  int b=blockIdx.x,t=threadIdx.x,l=t&63,w=__builtin_amdgcn_readfirstlane(t>>6);
  __shared__ u32 tm[8];
  u32x4 xv[2]; TS(22)
  const bool cal=b<256&&(b&127)==0; const int br=b<256?b:(b&1)*128;
  if(!cal){ UNR for(int i=0;i<2;i++) xv[i]=((const u32x4*)A.x)[(br*8+w*2+i)*64+l]; }
  else {
    if(l<4) st1c(rw,(l&1)*8192+(l>>1)*1024,b*16384+w*2048,SENT);
    vm0(); u32 mn[2]={~0u,~0u};
    UNR for(int i=0;i<NCAL;i++){ bar();
      u64 c0=__builtin_amdgcn_s_memtime(); asm volatile("s_waitcnt lgkmcnt(0)":::"memory");
      u32 v=ld1c(rw,(i&1)*8192+(i>>1)*1024,b*16384+w*2048); asm volatile("s_waitcnt vmcnt(0)":"+v"(v)::"memory");
      u64 c1=__builtin_amdgcn_s_memtime(); mn[i&1]=min(mn[i&1],(u32)(c1-c0)); }
    if(l<2) tm[l*4+w]=l?mn[1]:mn[0];
    __syncthreads();
    if(t==0) *(u32*)(ws+O_FL+F_SEL+(b>>7)*4)=min(min(tm[4],tm[5]),min(tm[6],tm[7]))<min(min(tm[0],tm[1]),min(tm[2],tm[3]))?8192u:0u;
  }
  if(b==192)
  UNR for(int i=0;i<4;i++){ u32x4 v={SENT,SENT,SENT,SENT}; int c=i*256+t;
    { int cc=c&511; const u32x2* p=(const u32x2*)A.mem0+((cc>>2)&7)*128+(cc>>6)*16+(cc&3)*4+((cc>>5)&1); u32x2 a=p[0],e=p[2]; v=u32x4{a.x,a.y,e.x,e.y}; }
    __builtin_amdgcn_raw_buffer_store_b128(v,rw,(b*1024+c)*16,0,PSC); }
  if(b>=4&&b<256&&t<16) ((u32x4*)(ws+O_FL))[b*16+t]=u32x4{0,0,0,0};
  if(!cal) UNR for(int i=0;i<2;i++){
    float f[8],s=0; UNR for(int j=0;j<4;j++){ f[2*j]=blo(xv[i][j]); f[2*j+1]=bhi(xv[i][j]); s+=f[2*j]+f[2*j+1]; }
    float mu=gsum(rsum16(s))*(1.f/512),q=0; UNR for(int j=0;j<8;j++){ f[j]-=mu; q+=f[j]*f[j]; }
    float rs=rsqrtf(gsum(rsum16(q))*(1.f/512)+1e-5f);
    int tk=br*8+w*2+i,tau=tk&31,kt=(tau>>2)&1,c=4*(tau>>3)+(tau&3);
    ((u32x4*)(ws+O_XF))[(((tk>>5)*2+kt)*16+(l>>2))*64+(l&3)*16+c]=u32x4{pk(f[0]*rs,f[1]*rs),pk(f[2]*rs,f[3]*rs),pk(f[4]*rs,f[5]*rs),pk(f[6]*rs,f[7]*rs)};
  }
  TS(23)
}
#define RG 63
#define PIN(x) asm volatile("":"+v"(x))
#define DL1 10
#define DL2 7
#define DL3 0
#define DL4 0
#define OPT 4
#define STR_(x) #x
#define STR(x) STR_(x)
#define CEXP 0.12751743f
typedef __attribute__((ext_vector_type(16))) u32 u32x16;
typedef __attribute__((ext_vector_type(32))) u32 u32x32;
#define MT(i)
#define MTACC
#define CLOB "v128","v129","v130","v131","v132","v133","v134","v135","v136","v137","v138","v139","v140","v141","v142","v143","v144","v145","v146","v147","v148","v149","v150","v151","v152","v153","v154","v155","v156","v157","v158","v159","v160","v161","v162","v163","v164","v165","v166","v167","v168","v169","v170","v171","v172","v173","v174","v175","v176","v177","v178","v179","v180","v181","v182","v183","v184","v185","v186","v187","v188","v189","v190","v191","v192","v193","v194","v195","v196","v197","v198","v199","v200","v201","v202","v203","v204","v205","v206","v207","v208","v209","v210","v211","v212","v213","v214","v215","v216","v217","v218","v219","v220","v221","v222","v223","v224","v225","v226","v227","v228","v229","v230","v231","v232","v233","v234","v235","v236","v237","v238","v239","v240","v241","v242","v243","v244","v245","v246","v247","s40","s41","s42","s43","s44","s45","s46","s47","s48","s49","s50","s51","s52","s53","s54","s55","s56","s57","s58","s59","s60","s61","s62","s63","s80","s81","s82","s83","s84","s85","s86","s87"
DI void roleR(const Args& A,char* ws,int h,int th,const int w,int& dead){
  const int l=lane(),g=l>>4,n=l&15;
  rsrc_t rw=mk(ws,WS_SZ);
  u32x32 wa[8],wv[2],wo[2];
  { rsrc_t rq=mk(A.wq,4*MB),rk=mk(A.wk,4*MB),rv=mk(A.wv,4*MB),ro=mk(A.wo,4*MB);
    const u32 rl=(32*(g>>1)+8*(g&1))*8192+h*256;
#define ROWJ(j) ((16*(((j)>>1)&1)+4*(((j)>>3)&1)+2*(((j)>>2)&1)+((j)&1))*8192)
    u32x2 v[32]; u32x4 u[16];
    UNR for(int j=0;j<32;j++) v[j]=ld2(rv,rl+n*8,(2*w+(j>>4))*64*8192+th*128+ROWJ(j));
    UNR for(int j=0;j<16;j++) u[j]=ld4(ro,g*8192+n*16,(h*128+th*64+(j&7)+32*(j>>3))*1024+w*256);
    const u32 base=w*32768,va=ROWJ(g)+(32*(n>>3)+8*((n>>2)&1))*8192+h*256+(n&3)*16; u32 ra=base+l*4; PIN(ra); u32x32 f;
    stream<16,8>(base,[&](int c,u32 d){ u32 so=((c+2*w)&7)*64*8192+w*64; asm volatile("":"+s"(so),"+s"(d));
        UNR for(int i=0;i<4;i++) dmaw(c<8?rq:rk,va,so+ROWJ(4*i),d+i*1024); },
      [&](int c,u32 o){ u32 p[16]; UNR for(int i=0;i<16;i++) p[i]=LDS(u32,ra+o+i*256);
        UNR for(int j=0;j<16;j++){ int q=(j&7)*2; f[(c&1)*16+j]=(j&8)?phi(p[q],p[q+1]):plo(p[q],p[q+1]); }
#define PA(i,r) if(c==2*i+1) asm volatile("":"={a[" r "]}"(wa[i]):"0"(f));
        PA(0,"0:31") PA(1,"32:63") PA(2,"64:95") PA(3,"96:127") PA(4,"128:159") PA(5,"160:191") PA(6,"192:223") PA(7,"224:255") });
    bar();
    UNR for(int q=0;q<8;q++) UNR for(int j=0;j<8;j++){ const u32x2* p=v+(q>>2)*16+2*j; int hi=(q>>1)&1;
      wv[q>>2][(q&3)*8+j]=(q&1)?phi(p[0][hi],p[1][hi]):plo(p[0][hi],p[1][hi]); }
    asm volatile("":"+{v[0:31]}"(wv[0]),"+{v[32:63]}"(wv[1])); SB();
    UNR for(int q=0;q<8;q++) UNR for(int j=0;j<8;j++) wo[q>>2][(q&3)*8+j]=(q&1)?phi(u[2*j][q>>1],u[2*j+1][q>>1]):plo(u[2*j][q>>1],u[2*j+1][q>>1]);
    asm volatile("":"+{v[64:95]}"(wo[0]),"+{v[96:127]}"(wo[1]));
  }
  const u32 sel0=__builtin_amdgcn_readfirstlane(ld1(rw,0,O_FL+F_SEL)),sel1=__builtin_amdgcn_readfirstlane(ld1(rw,0,O_FL+F_SEL+4));
  const int pi=2*h+th,p=(n>>1)&1; const u32 l16=l*16;
  { u32x4 sel; UNR for(int j=0;j<4;j++) sel[j]=(n&3)==j?((n&4)?0x3F800000u:0x3F80u):0u; LDS(u32x4,17408+l16)=sel; LDS(u32x4,18432)=u32x4{0x3F803F80u,0x3F803F80u,0x3F80u,0}; }
  const int rho=(2*w+(n>>3))*8+g;
  u32x8 vc={p?0xEEEEu:0x4444u, l16, rho*16384+((pi+rho+(rho&3))&63)*128+(n&7)*16u, 8192+2*g*288+w*64+n*4u,
            8192+(2*(n>>2)+(n&1))*288+g*16u, 32768+l16, w*2048+((g>>1)*4+(n>>2))*128+(n&1)*64+(2*(g&1)+p)*16u,
            w==0?g*64+n*8u:w==3?10496+(4*th+g)*288+n*16u:13312+(w-1)*2048+l16};
  const u32 kvs=O_KV+h*16384+w*3072;
  TS(1) waitf(rw,O_FL+F_RI,64,1,dead,128); waitf(rw,O_FL+F_KV+2*h*128,1,1,dead); TS(2) TC(20,__builtin_amdgcn_s_memtime())
  UNR for(int i=0;i<3;i++) dma16(rw,l16,kvs+i*1024+(3*w+i>=8?th*4096:0),32768+(3*w+i)*1024);
  u32x16 sc={(u32)w, sel0, MB+sel0, 2*MB+sel1, O_M+sel1, pi*16384u, O_KV+h*16384+MB/2+(w-1+(w==3?th:0))*4096, pi*16384u+(w==3?5120:(w-1)*3072),
             O_FL+F_KV+(2*h+1)*128u, O_FL+F_KM+pi*128u, w==3?O_KM+h*2048+th*1024:O_VM+pi*4096+(w-1)*2048u, w==3?65536u:262144u,
             49152+(w-1)*4096u, (u32)dead, 0, 0};
  u32x8 pc={};
  asm volatile(
".macro SQ c,a,w\n"
" v_smfmac_f32_16x16x64_bf16 v[\\c:\\c+3],v[\\a:\\a+3],a[\\w:\\w+7],v248\n"
".endm\n"
".macro SV c,a,w\n"
" v_smfmac_f32_16x16x64_bf16 v[\\c:\\c+3],v[\\a:\\a+3],v[\\w:\\w+7],v248\n"
".endm\n"
".macro MZ d,a,b\n"
" v_mfma_f32_16x16x32_bf16 v[\\d:\\d+3],v[\\a:\\a+3],v[\\b:\\b+3],0\n"
".endm\n"
".macro MA d,a,b\n"
" v_mfma_f32_16x16x32_bf16 v[\\d:\\d+3],v[\\a:\\a+3],v[\\b:\\b+3],v[\\d:\\d+3]\n"
".endm\n"
".macro RD d,a,o\n"
" ds_read_b128 v[\\d:\\d+3],v\\a offset:\\o\n"
".endm\n"
".macro AD2 c\n"
" v_add_f32 v[\\c],v[\\c],v[\\c+2]\n"
" v_add_f32 v[\\c+1],v[\\c+1],v[\\c+3]\n"
".endm\n"
".macro CV d,a,b\n"
" v_cvt_pk_bf16_f32 v[\\d],v[\\a],v[\\b]\n"
".endm\n"
".macro ZR a,n\n"
" .set zi,\\a\n"
" .rept \\n\n"
" v_mov_b64 v[zi:zi+1],0\n"
" .set zi,zi+2\n"
" .endr\n"
".endm\n"
".macro SPIN lab,inc\n"
" s_cmp_eq_u32 s80,0\n"
" s_cselect_b32 s81,\\inc,0\n"
" s_add_u32 s79,s79,s81\n"
" s_add_u32 s80,s80,1\n"
" s_cmp_gt_u32 s80," STR(SPINCAP) "\n"
" s_cselect_b32 s77,1,s77\n"
" s_cmp_lg_u32 s77,0\n"
" s_cbranch_scc1 \\lab\n"
".endm\n"
".macro EXPORT\n"
" RD 144,255,0\n"
" RD 148,255,1024\n"
" s_waitcnt lgkmcnt(0)\n"
" buffer_store_dwordx4 v[144:147],v249,%3,s74 offen sc1\n"
" s_cmp_eq_u32 s64,3\n"
" s_cbranch_scc1 1f\n"
" buffer_store_dwordx4 v[148:151],v249,%3,s74 offen offset:1024 sc1\n"
"1:\n"
".endm\n"
" s_mov_b32 s56,0xCCCCCCCC\n"
" s_mov_b32 s57,0xCCCCCCCC\n"
" s_lshl_b32 s58,s64,11\n"
" s_add_u32 s59,s58,2048\n"
" s_and_b32 s59,s59,8191\n"
" s_xor_b32 s60,s58,4096\n"
" s_xor_b32 s61,s59,4096\n"
" s_lshr_b32 s63,s64,1\n"
" s_lshl_b32 s63,s63,10\n"
" s_and_b32 s82,s64,1\n"
" s_lshl_b32 s82,s82,2\n"
" s_add_u32 s63,s63,s82\n"
" s_mov_b32 s87,0x3e0293ee\n"
" ZR 168,16\n"
" ZR 216,16\n"
" v_mov_b32 v203,0\n"
" s_waitcnt vmcnt(0) lgkmcnt(0)\n"
"RL_loop:\n"
" v_add_u32 v160,s58,v249\n"
" v_add_u32 v161,s59,v249\n"
" v_add_u32 v162,s60,v249\n"
" v_add_u32 v163,s61,v249\n"
" s_add_u32 s84,s65,65536\n"
" s_mov_b32 m0,s76\n"
" s_add_u32 s86,s78,2\n"
" s_min_u32 s86,s86,62\n"
" s_cmp_eq_u32 s64,0\n"
" s_cselect_b32 s86,0,s86\n"
" v_mov_b32 v202,s78\n"
" v_mov_b64 v[204:205],-1\n"
" v_mov_b64 v[206:207],-1\n"
" s_add_u32 s82,s67,s69\n"
" s_add_u32 s82,s82,s58\n"
" s_mov_b32 s80,0\n"
MT(40)
" buffer_load_dwordx4 v[128:131],v254,%3,s68 offen sc1\n"
" buffer_load_dwordx4 v[132:135],v254,%3,s68 offen offset:1024 sc1\n"
" s_waitcnt vmcnt(0)\n"
"RL_retry:\n"
MT(42)
" ds_write_b128 v160,v[128:131]\n"
" SQ 168,128,0\n"
" ds_write_b128 v160,v[132:135] offset:1024\n"
" v_max3_u32 v200,v128,v129,v130\n"
" SQ 172,128,8\n"
" v_max3_u32 v201,v131,v132,v133\n"
" v_max3_u32 v200,v200,v134,v135\n"
" SQ 168,132,16\n"
" v_max_u32 v200,v200,v201\n"
" v_cmp_eq_u32 vcc,-1,v200\n"
" s_cbranch_vccnz RL_failm\n"
"RL_okm:\n"
" SV 184,128,0\n"
" v_add_u32 v201,s63,v249\n"
" SV 184,132,32\n"
" v_readfirstlane_b32 s85,v203\n"
" SV 188,128,8\n"
" SV 188,132,40\n"
" SV 192,128,16\n"
" AD2 184\n"
" SV 192,132,48\n"
" CV 184,184,185\n"
" SV 196,128,24\n"
" AD2 188\n"
" SV 196,132,56\n"
" CV 188,188,189\n"
" s_cmp_lg_u32 s64,3\n"
" s_waitcnt lgkmcnt(0)\n"
" s_barrier\n"
" RD 136,161,0\n"
" RD 140,161,1024\n"
" SQ 172,132,24\n"
" RD 144,162,0\n"
" SQ 176,128,128\n"
" RD 148,162,1024\n"
" SQ 180,128,136\n"
" RD 152,163,0\n"
" SQ 176,132,144\n"
" RD 156,163,1024\n"
" SQ 180,132,152\n"
" s_cbranch_scc1 3f\n"
" buffer_store_dword v202,off,%3,s73 sc1\n"
"3:\n"
" AD2 192\n"
" s_waitcnt lgkmcnt(5)\n"
" SQ 168,136,32\n"
" AD2 196\n"
" SQ 172,136,40\n"
" CV 192,192,193\n"
" CV 196,196,197\n"
" s_waitcnt lgkmcnt(0)\n"
" SQ 168,140,48\n"
" .if (" STR(OPT) "&20)==4\n"
" buffer_store_dwordx4 v[204:207],v249,%3,s82 offen sc1\n"
" .endif\n"
" ds_write_b32 v201,v184 offset:13312\n"
" SQ 172,140,56\n"
" .if " STR(OPT) "&8\n"
" s_cmp_lt_u32 s85,s86\n"
" s_cbranch_scc1 RL_failf\n"
"RL_okf:\n"
" s_cmp_eq_u32 s64,0\n"
" .endif\n"
" ds_write_b32 v201,v188 offset:15360\n"
" SQ 168,144,64\n"
" ds_write_b32 v201,v192 offset:13320\n"
" SQ 172,144,72\n"
" .if " STR(OPT) "&8\n"
" s_cbranch_scc1 5f\n"
" buffer_load_dwordx4 v249,%3,s70 offen offset:0 sc1 lds\n"
"5:\n"
" .endif\n"
" RD 128,253,0\n"
" SQ 168,148,80\n"
" .if (" STR(OPT) "&20)==4\n"
" buffer_store_dwordx4 v[204:207],v249,%3,s82 offen offset:1024 sc1\n"
" .endif\n"
" RD 132,253,4096\n"
" SQ 172,148,88\n"
" ds_write_b32 v201,v196 offset:15368\n"
" SQ 168,152,96\n"
" .if " STR(OPT) "&8\n"
" s_cbranch_scc1 5f\n"
" buffer_load_dwordx4 v249,%3,s70 offen offset:1024 sc1 lds\n"
"5:\n"
" .endif\n"
" RD 160,253,1024\n"
" SQ 168,156,112\n"
" RD 164,253,5120\n"
" SQ 172,152,104\n"
" .if " STR(OPT) "&8\n"
" s_cbranch_scc1 5f\n"
" buffer_load_dwordx4 v249,%3,s70 offen offset:2048 sc1 lds\n"
"5:\n"
" .endif\n"
" RD 184,253,2048\n"
" SQ 172,156,120\n"
" RD 188,253,6144\n"
" SQ 176,136,160\n"
" AD2 168\n"
" SQ 180,136,168\n"
" .if " STR(OPT) "&8\n"
" s_cbranch_scc1 5f\n"
" buffer_load_dwordx4 v249,%3,s70 offen offset:3072 sc1 lds\n"
"5:\n"
" .endif\n"
" RD 192,253,3072\n"
" SQ 176,140,176\n"
" AD2 172\n"
" SQ 180,140,184\n"
" CV 168,168,172\n"
" CV 169,169,173\n"
" SQ 176,144,192\n"
" ds_write2_b32 v251,v168,v169 offset1:72\n"
" SQ 180,144,200\n"
" RD 196,253,7168\n"
" SQ 176,148,208\n"
" .if " STR(OPT) "&16\n"
" buffer_store_dwordx4 v[204:207],v249,%3,s82 offen sc1\n"
" .endif\n"
" SQ 180,148,216\n"
" .if " STR(OPT) "&16\n"
" buffer_store_dwordx4 v[204:207],v249,%3,s82 offen offset:1024 sc1\n"
" .endif\n"
" s_waitcnt lgkmcnt(1)\n"
" s_barrier\n"
" RD 200,252,0\n"
" SQ 176,152,224\n"
" RD 204,252,64\n"
" SQ 176,156,240\n"
" RD 208,252,128\n"
" SQ 180,152,232\n"
" RD 212,252,192\n"
" s_mov_b64 vcc,s[56:57]\n"
" SQ 180,156,248\n"
MT(44)
" s_cmp_eq_u32 s78,63\n"
" AD2 176\n"
" s_waitcnt lgkmcnt(3)\n"
" MZ 168,128,200\n"
" MZ 172,132,200\n"
" s_waitcnt lgkmcnt(2)\n"
" AD2 180\n"
" MA 168,160,204\n"
" CV 176,176,180\n"
" CV 177,177,181\n"
" ds_write_b32 v251,v176 offset:2304\n"
" ds_write_b32 v251,v177 offset:2592\n"
" MA 172,164,204\n"
" s_waitcnt lgkmcnt(2)\n"
" MA 168,184,208\n"
" MA 172,188,208\n"
" s_waitcnt lgkmcnt(0)\n"
" s_barrier\n"
" s_cbranch_scc1 RL_last\n"
" RD 136,252,2304\n"
" RD 140,252,2368\n"
" MA 168,192,212\n"
" RD 144,252,2432\n"
" RD 148,252,2496\n"
" MA 172,196,212\n"
" RD 152,253,8192\n"
" RD 156,253,9216\n"
" RD 128,253,10240\n"
" RD 132,253,11264\n"
" RD 196,216,18432\n"
" s_waitcnt lgkmcnt(8)\n"
" MZ 176,136,200\n"
" s_nop 0\n"
" v_cndmask_b32 v168,v168,v172,vcc\n"
" v_cndmask_b32 v169,v169,v173,vcc\n"
" s_waitcnt lgkmcnt(7)\n"
" MA 176,140,204\n"
" v_cndmask_b32 v170,v170,v174,vcc\n"
" v_cndmask_b32 v171,v171,v175,vcc\n"
" s_waitcnt lgkmcnt(5)\n"
" MA 176,144,208\n"
" v_max3_f32 v180,v168,v169,v170\n"
" v_max_f32 v180,v180,v171\n"
" MA 176,148,212\n"
" RD 200,249,13312\n"
" RD 204,249,14336\n"
" v_max_f32_dpp v180,v180,v180 quad_perm:[2,3,0,1] row_mask:0xf bank_mask:0xf\n"
" RD 208,249,15360\n"
" RD 212,249,16384\n"
" s_nop 2\n"
MT(46)
" v_max3_f32 v181,v180,v176,v177\n"
" v_max3_f32 v180,v180,v176,v177\n"
" v_cndmask_b32 v182,v176,v177,vcc\n"
" s_nop 0\n"
" v_permlane16_swap_b32 v180,v181\n"
" v_max_f32 v183,v180,v181\n"
" v_max_f32 v180,v180,v181\n"
" s_nop 1\n"
" v_permlane32_swap_b32 v180,v183\n"
" v_max_f32 v180,v180,v183\n"
" v_mul_f32 v180,s87,v180\n"
" v_fma_f32 v168,v168,s87,-v180\n"
" v_fma_f32 v169,v169,s87,-v180\n"
" v_exp_f32 v168,v168\n"
" v_fma_f32 v170,v170,s87,-v180\n"
" v_exp_f32 v169,v169\n"
" v_fma_f32 v171,v171,s87,-v180\n"
" v_exp_f32 v170,v170\n"
" v_exp_f32 v171,v171\n"
" CV 160,168,169\n"
" CV 161,170,171\n"
" v_cndmask_b32 v172,v160,v216,vcc\n"
" v_cndmask_b32 v173,v161,v216,vcc\n"
" v_cndmask_b32_dpp v174,v160,v216,vcc quad_perm:[2,3,0,1] row_mask:0xf bank_mask:0xf\n"
" v_cndmask_b32_dpp v175,v161,v216,vcc quad_perm:[2,3,0,1] row_mask:0xf bank_mask:0xf\n"
" v_fma_f32 v182,v182,s87,-v180\n"
" s_waitcnt lgkmcnt(0)\n"
" MZ 184,152,172\n"
" v_exp_f32 v182,v182\n"
" v_mov_b32_dpp v164,v172 quad_perm:[2,3,0,1] row_mask:0xf bank_mask:0xf\n"
" MZ 188,156,172\n"
" v_mov_b32_dpp v165,v173 quad_perm:[2,3,0,1] row_mask:0xf bank_mask:0xf\n"
" v_mov_b32_dpp v166,v174 quad_perm:[2,3,0,1] row_mask:0xf bank_mask:0xf\n"
" v_mov_b32_dpp v167,v175 quad_perm:[2,3,0,1] row_mask:0xf bank_mask:0xf\n"
" v_mov_b32_dpp v181,v182 quad_perm:[2,3,0,1] row_mask:0xf bank_mask:0xf\n"
" CV 162,182,181\n"
" MA 184,128,164\n"
" v_cndmask_b32 v168,v162,v216,vcc\n"
" v_cndmask_b32 v169,v162,v216,vcc\n"
" MZ 192,196,160\n"
" v_mov_b32_dpp v170,v168 quad_perm:[2,3,0,1] row_mask:0xf bank_mask:0xf\n"
" v_mov_b32_dpp v171,v168 quad_perm:[2,3,0,1] row_mask:0xf bank_mask:0xf\n"
" MA 188,132,164\n"
" s_nop 0\n"
" MA 184,200,168\n"
" MA 184,204,168\n"
" v_add_f32_dpp v192,v192,v192 quad_perm:[2,3,0,1] row_mask:0xf bank_mask:0xf\n"
" MA 188,208,168\n"
" v_rcp_f32 v192,v192\n"
" MA 188,212,168\n"
" s_nop 3\n"
" v_mul_f32 v184,v184,v192\n"
" v_mul_f32 v185,v185,v192\n"
" v_mul_f32 v186,v186,v192\n"
" v_mul_f32 v187,v187,v192\n"
" v_mul_f32 v188,v188,v192\n"
" v_mul_f32 v189,v189,v192\n"
" v_mul_f32 v190,v190,v192\n"
" v_mul_f32 v191,v191,v192\n"
" CV 172,184,188\n"
" CV 173,185,189\n"
" CV 174,186,190\n"
" CV 175,187,191\n"
MT(48)
" s_nop 1\n"
" SV 216,172,64\n"
" SV 220,172,72\n"
" SV 224,172,80\n"
" SV 228,172,88\n"
" AD2 216\n"
" CV 128,216,217\n"
" SV 232,172,96\n"
" AD2 220\n"
" CV 129,220,221\n"
" SV 236,172,104\n"
" AD2 224\n"
" CV 130,224,225\n"
" SV 240,172,112\n"
" AD2 228\n"
" CV 131,228,229\n"
" SV 244,172,120\n"
" buffer_store_dwordx4 v[128:131],v250,%3,s65 offen sc1\n"
" AD2 232\n"
" AD2 236\n"
" AD2 240\n"
" CV 132,232,233\n"
" AD2 244\n"
" CV 133,236,237\n"
" CV 134,240,241\n"
" CV 135,244,245\n"
" buffer_store_dwordx4 v[132:135],v250,%3,s84 offen sc1\n"
MT(50)
" RD 160,249,17408\n"
" ZR 168,16\n"
" ZR 216,16\n"
" s_add_u32 s68,s68,16384\n"
" s_cmp_lg_u32 s64,0\n"
" s_cbranch_scc1 RL_other\n"
" s_add_u32 s82,s65,s69\n"
" s_add_u32 s83,s82,4096\n"
" s_lshr_b32 s84,s69,7\n"
" s_add_u32 s84,s84,s68\n"
" s_mov_b32 s80,0\n"
" s_waitcnt lgkmcnt(0)\n"
" s_sleep " STR(DL1) "\n"
MT(52)
"RL_red:\n"
" buffer_load_dwordx4 v[128:131],v249,%3,s82 offen sc1\n"
" buffer_load_dwordx4 v[132:135],v249,%3,s83 offen sc1\n"
" buffer_load_dwordx4 v[136:139],v249,%3,s82 offen offset:1024 sc1\n"
" buffer_load_dwordx4 v[140:143],v249,%3,s83 offen offset:1024 sc1\n"
" buffer_load_dwordx4 v[144:147],v249,%3,s82 offen offset:2048 sc1\n"
" buffer_load_dwordx4 v[148:151],v249,%3,s83 offen offset:2048 sc1\n"
" buffer_load_dwordx4 v[152:155],v249,%3,s82 offen offset:3072 sc1\n"
" buffer_load_dwordx4 v[156:159],v249,%3,s83 offen offset:3072 sc1\n"
" s_waitcnt vmcnt(7)\n"
" MZ 164,160,128\n"
" s_waitcnt vmcnt(6)\n"
" MA 164,160,132\n"
" s_waitcnt vmcnt(5)\n"
" MA 164,160,136\n"
" s_waitcnt vmcnt(4)\n"
" MA 164,160,140\n"
" s_waitcnt vmcnt(3)\n"
" MA 164,160,144\n"
" s_waitcnt vmcnt(2)\n"
" MA 164,160,148\n"
" s_waitcnt vmcnt(1)\n"
" MA 164,160,152\n"
" s_waitcnt vmcnt(0)\n"
" MA 164,160,156\n"
" s_nop 7\n"
" v_cmp_u_f32 vcc,v164,v164\n"
" v_add_f32_dpp v164,v164,v164 row_ror:8 row_mask:0xf bank_mask:0xf\n"
" v_add_f32_dpp v165,v165,v165 row_ror:8 row_mask:0xf bank_mask:0xf\n"
" v_add_f32_dpp v166,v166,v166 row_ror:8 row_mask:0xf bank_mask:0xf\n"
" v_add_f32_dpp v167,v167,v167 row_ror:8 row_mask:0xf bank_mask:0xf\n"
" s_cbranch_vccnz RL_failr\n"
"RL_okr:\n"
" s_mov_b64 exec,0x00FF00FF\n"
" CV 164,164,165\n"
" CV 165,166,167\n"
" buffer_store_dwordx2 v[164:165],v255,%3,s84 offen sc1\n"
" s_mov_b64 exec,-1\n"
"RL_join:\n"
MT(54)
" s_barrier\n"
" s_add_u32 s78,s78,1\n"
" s_mov_b32 s81,s67\n"
" s_mov_b32 s67,s65\n"
" s_mov_b32 s65,s66\n"
" s_mov_b32 s66,s81\n"
" v_xor_b32 v253,16384,v253\n"
MTACC
" s_sleep " STR(DL2) "\n"
" s_branch RL_loop\n"
"RL_other:\n"
" v_mov_b64 v[164:165],-1\n"
" v_mov_b64 v[166:167],-1\n"
" s_add_u32 s82,s67,s71\n"
" .if (" STR(OPT) "&8)==0\n"
" s_cmp_lt_u32 s85,s86\n"
" s_cbranch_scc1 RL_failf\n"
"RL_okf:\n"
" .endif\n"
" s_xor_b32 s72,s72,128\n"
" s_sleep " STR(DL3) "\n"
" .if (" STR(OPT) "&9)==0\n"
" buffer_load_dwordx4 v249,%3,s70 offen sc1 lds\n"
" buffer_load_dwordx4 v249,%3,s70 offen offset:1024 sc1 lds\n"
" buffer_load_dwordx4 v249,%3,s70 offen offset:2048 sc1 lds\n"
" buffer_load_dwordx4 v249,%3,s70 offen offset:3072 sc1 lds\n"
" .endif\n"
" buffer_load_dword v203,off,%3,s72 sc1\n"
" s_sleep " STR(DL4) "\n"
" .if (" STR(OPT) "&2)==0\n"
" EXPORT\n"
" .endif\n"
" .if (" STR(OPT) "&4)==0\n"
" buffer_store_dwordx4 v[164:167],v249,%3,s82 offen sc1\n"
" buffer_store_dwordx4 v[164:167],v249,%3,s82 offen offset:1024 sc1\n"
" buffer_store_dwordx4 v[164:167],v249,%3,s82 offen offset:2048 sc1\n"
" .endif\n"
" s_add_u32 s70,s70,0x80000\n"
" s_add_u32 s74,s74,s75\n"
" .if " STR(RG) "==7\n"
" s_bitset0_b32 s70,22\n"
" s_lshl_b32 s81,s75,3\n"
" s_andn2_b32 s74,s74,s81\n"
" .endif\n"
" s_xor_b32 s76,s76,16384\n"
" s_branch RL_join\n"
"RL_failm:\n"
" SPIN RL_okm,1\n"
" buffer_load_dwordx4 v[128:131],v254,%3,s68 offen sc1\n"
" buffer_load_dwordx4 v[132:135],v254,%3,s68 offen offset:1024 sc1\n"
" ZR 168,4\n"
" s_waitcnt vmcnt(0)\n"
" s_branch RL_retry\n"
"RL_failr:\n"
" .set zi,129\n"
" .rept 15\n"
" v_max3_u32 v128,v128,v[zi],v[zi+1]\n"
" .set zi,zi+2\n"
" .endr\n"
" v_max_u32 v128,v128,v159\n"
" v_cmp_eq_u32 vcc,-1,v128\n"
" s_cbranch_vccz RL_okr\n"
" SPIN RL_okr,1024\n"
" s_branch RL_red\n"
"RL_failf:\n"
" SPIN RL_okf,0x100000\n"
" buffer_load_dword v200,off,%3,s72 sc1\n"
" s_waitcnt vmcnt(0)\n"
" v_readfirstlane_b32 s85,v200\n"
" s_nop 3\n"
" s_cmp_lt_u32 s85,s86\n"
" s_cbranch_scc1 RL_failf\n"
" s_branch RL_okf\n"
"RL_last:\n"
" s_cmp_eq_u32 s64,0\n"
" s_cbranch_scc1 2f\n"
" s_waitcnt lgkmcnt(0)\n"
" EXPORT\n"
"2:\n"
" v_mov_b32 v202,64\n"
" s_waitcnt vmcnt(0) lgkmcnt(0)\n"
" s_barrier\n"
" buffer_store_dword v202,off,%3,s73 sc1\n"
" s_nop 7\n"
  :"+{s[64:79]}"(sc),"+{v[248:255]}"(vc),"+{s[88:95]}"(pc)
  :"s"(rw),"{a[0:31]}"(wa[0]),"{a[32:63]}"(wa[1]),"{a[64:95]}"(wa[2]),"{a[96:127]}"(wa[3]),"{a[128:159]}"(wa[4]),"{a[160:191]}"(wa[5]),"{a[192:223]}"(wa[6]),"{a[224:255]}"(wa[7]),
   "{v[0:31]}"(wv[0]),"{v[32:63]}"(wv[1]),"{v[64:95]}"(wo[0]),"{v[96:127]}"(wo[1])
  :"memory","scc","vcc",CLOB);
  dead=sc[13];
  TC(21,__builtin_amdgcn_s_memtime()) TS(3) TC(9,sc[15]&1023) TC(10,(sc[15]>>10)&1023)
}
#define SPZ 0
#define DY 3
#define DW 4
#define GL __attribute__((address_space(1)))
#define XB 0
#define DH 8
#define DZ 24
DI u32 mx4(u32 m,u32x4 v){ return max(max(max(m,v.x),v.y),max(v.z,v.w)); }
DI u32x4 pk8(f32x4 a,f32x4 b){ return u32x4{pk(a.x,b.x),pk(a.y,b.y),pk(a.z,b.z),pk(a.w,b.w)}; }
DI u32x4 pkc(f32x4 a,f32x4 b){ return u32x4{pk(a.x,a.y),pk(a.z,a.w),pk(b.x,b.y),pk(b.z,b.w)}; }
DI void roleB1(const Args& A,char* ws,int h,int par,const int w,int& dead){
  const int l=lane(),g=l>>4,n=l&15;
  rsrc_t rw=mk(ws,WS_SZ); const u32 l16=l*16;
  enum{LA=3,L_B=65536,L_M=139264};
  UNR for(int i=0;i<8;i++) dma16(rw,l16,O_XF+par*32768+(w*8+i)*1024,(w*8+i)*1024);
  u32x4 wq[16][2],wk[16][2],wv[16][2];
  { rsrc_t rk=mk(A.wk,4*MB),rv=mk(A.wv,4*MB),rq=mk(A.wq,4*MB); const u32 base=32768+w*28672;
    const u32 va=g*8192+(n>>2)*65536+(n&3)*16,vb=(l>>5)*8192+((l>>3)&3)*65536+(l&7)*16; u32 ra=base+l*4,rb=base+l*8+(w&1)*4; PIN(ra); PIN(rb);
    stream<32,7>(base,[&](int c,u32 d){ u32 so=h*256; asm volatile("":"+s"(so),"+s"(d));
        UNR for(int i=0;i<4;i++){ if(c>=8&&c<24) dmaw(rv,vb,so+(w>>1)*128+((c-8)*32+2*i)*8192,d+i*1024);
          else dmaw(c<8?rk:rq,va,so+w*64+((c&7)*64+(i>>1)*32+(i&1)*4)*8192,d+i*1024); } },
      [&](int c,u32 o){ u32 t[16]; u32x4 f0,f1;
        if(c>=8&&c<24){ UNR for(int i=0;i<8;i++) t[i]=LDS(u32,rb+o+i*512); mkfr(t,f0,f1);
          if(c<17){ f0=toA(f0); f1=toA(f1); } else { PIN(f0); PIN(f1); } wv[c-8][0]=f0; wv[c-8][1]=f1; }
        else { UNR for(int i=0;i<16;i++) t[i]=LDS(u32,ra+o+i*256);
          UNR for(int q=0;q<2;q++){ mkfr(t+8*q,f0,f1); if(c<8){ wk[2*c+q][0]=toA(f0); wk[2*c+q][1]=toA(f1); } else { PIN(f0); PIN(f1); wq[2*(c&7)+q][0]=f0; wq[2*(c&7)+q][1]=f1; } } } }); }
  int bi=0,cf=0,cg=0; const u32 fo=O_FL+F_O+((par*2+(w&1))*32+h)*128+(w>>1)*4; vm0(); TS(1)
  for(int i=0;i<32+LA;i++){
    const int s=2*i+par,sa=s-2*LA; const u32 lb=L_B+bi*24576+l16; u32x4 pq[2],pk_[2],pv[2];
    bool pub=i>LA; u32 fl=pub?LDS(u32,151552+w*1024+l16):0; bar();
    if(i>=LA){
      if(__any(fl<(u32)sa+1)){ if(pub){ vm0(); st1c(rw,0,fo,sa-1); pub=false; } waitf(rw,O_FL+F_KM+2*h*128,2,sa+1,dead,128); CNT(cf,1) }
      if(i==32) TS(2) if(i==34) TS(3)
    }
#define KMD(j) { if(j==0) dma16(rw,(2*(n>>2)+(n&1))*256+g*16,O_KM+((sa&RG)*32+h)*2048+w*64,L_M+w*1024); \
      else dma16(rw,l16,O_VM+((sa&RG)*32+h)*8192+(2*w+j-1)*1024,L_M+4096+(2*w+j-1)*1024); }
    if(i<32){
      f32x4 cq[2][2]={},ck[2][2]={},cv[2][2]={}; u32 xs=O_XF+(s+2)*32768+w*8192,xd=((i+1)&1)*32768+w*8192;
      u32x4 xx[2][2]; const u32 xb=(i&1)*32768+l16; xx[0][0]=LDS(u32x4,xb); xx[0][1]=LDS(u32x4,xb+16384);
      UNR for(int ks=0;ks<16;ks++){
        UNR for(int kt=0;kt<2;kt++){ u32x4 x=xx[ks&1][kt];
          ck[kt][0]=mfma(wk[ks][0],x,ck[kt][0]); SB(); xx[(ks+1)&1][kt]=LDS(u32x4,xb+(((ks+1)&15)+16*kt)*1024); SB();
          cv[kt][0]=mfma(x,wv[ks][0],cv[kt][0]); ck[kt][1]=mfma(wk[ks][1],x,ck[kt][1]); cv[kt][1]=mfma(x,wv[ks][1],cv[kt][1]); SB(); }
        if(ks<3) KMD(ks) else if(ks<11){ dma16(rw,l16,xs,xd); xs+=1024; xd+=1024; asm volatile("":"+s"(xs),"+s"(xd)); }
        SB(); }
      const u32 gb=O_KV+((s&RG)*32+h)*16384;
      UNR for(int ks=0;ks<16;ks++){ const int j=ks>>3,q=ks&3;
        UNR for(int kt=0;kt<2;kt++){ u32x4 x=xx[ks&1][kt];
          cq[kt][0]=mfma(wq[ks][0],x,cq[kt][0]); SB(); if(ks<15) xx[(ks+1)&1][kt]=LDS(u32x4,xb+(ks+1+16*kt)*1024); SB();
          cq[kt][1]=mfma(wq[ks][1],x,cq[kt][1]); SB();
          if(kt==0){ if(ks&4) pv[j][q]=pk(cv[q>>1][j][(q&1)*2],cv[q>>1][j][(q&1)*2+1]); else pk_[j][q]=pk(ck[j][0][q],ck[j][1][q]); SB(); } }
        if(ks==3){ vm0(); if(pub) st1c(rw,0,fo,sa-1); pub=false; }
        if(q==3){ if(ks&4) st4c(rw,l16,gb+8192+(2*w+j)*1024,pv[j]); else st4c(rw,l16,gb+(j*4+w)*1024,pk_[j]); }
        SB(); }
      UNR for(int j=0;j<2;j++) pq[j]=toA(pk8(cq[j][0],cq[j][1]));

    } else { KMD(0) KMD(1) KMD(2) }
    if(i>=LA){
      const int qt=w&1,eh=w>>1; f32x4 s0={},s1={},sm={}; u32x4 qf[4],kf[4];
      UNR for(int k=0;k<4;k++){ qf[k]=LDS(u32x4,lb+16384+(qt*4+k)*1024); kf[k]=LDS(u32x4,lb+k*1024); } SB();
      UNR for(int k=0;k<4;k++){ s0=mfma(kf[k],qf[k],s0); SB(); kf[k]=LDS(u32x4,lb+(4+k)*1024); SB(); }
      UNR for(int k=0;k<4;k++) s1=mfma(kf[k],qf[k],s1);
      SB(); if(i>=32) vm0();
      if(pub) st1c(rw,0,fo,sa-1);
      dma16(rw,(l16&16)*8,O_FL+F_KM+2*h*128,151552+w*1024); bar();
      UNR for(int k=0;k<4;k++) kf[k]=LDS(u32x4,L_M+k*1024+l16); SB();
      UNR for(int k=0;k<4;k++){ sm=mfma(kf[k],qf[k],sm); SB(); kf[k]=LDS(u32x4,lb+8192+(eh*4+k)*1024); qf[k]=LDS(u32x4,L_M+4096+(eh*4+k)*1024+l16); SB(); }
      const int dq=8*(n>>2)+4*qt+(n&3)-8*g; float pe[10],mx=fmaxf(sm.x,sm.y)*CEXP;
      UNR for(int j=0;j<8;j++){ pe[j]=j<=dq?(j<4?s0[j&3]:s1[j&3])*CEXP:-1.4426950e9f; mx=fmaxf(mx,pe[j]); }
      pe[8]=sm.x*CEXP; pe[9]=sm.y*CEXP; mx=gmax(mx); float su=0;
      UNR for(int j=0;j<10;j++){ pe[j]=__builtin_amdgcn_exp2f(pe[j]-mx); su+=pe[j]; }
      su=__builtin_amdgcn_rcpf(gsum(su));
      u32x4 pf={pk(pe[0],pe[1]),pk(pe[2],pe[3]),pk(pe[4],pe[5]),pk(pe[6],pe[7])}; u32 p1=pk(pe[8],pe[9]); u32x4 pm={p1,p1,p1,p1};
      f32x4 d[4];
      UNR for(int t=0;t<4;t++){ d[t]=mfma(kf[t],pf,f32x4{0,0,0,0});
        d[t]=mfma(u32x4{qf[(t&1)*2][t&2],qf[(t&1)*2][(t&2)+1],qf[(t&1)*2+1][t&2],qf[(t&1)*2+1][(t&2)+1]},pm,d[t])*su; }
      SB(); vm0();
      UNR for(int j=0;j<2;j++) st4c(rw,l16,O_OF+((2*sa+qt)*128+h*4+eh*2+j)*1024,
        u32x4{pk(d[0][2*j],d[1][2*j]),pk(d[2][2*j],d[3][2*j]),pk(d[0][2*j+1],d[1][2*j+1]),pk(d[2][2*j+1],d[3][2*j+1])});
    } else vm0();
    if(i<32){ bar();
      if(w==0) st1c(rw,0,O_FL+F_KV+(2*h+par)*128,s+1);
      UNR for(int j=0;j<2;j++){ LDS(u32x4,lb+(j*4+w)*1024)=pk_[j]; LDS(u32x4,lb+8192+(2*w+j)*1024)=pv[j]; LDS(u32x4,lb+16384+(j*4+w)*1024)=pq[j]; } }
    bi=bi==LA-1?0:bi+1;
  }
  vm0(); st1c(rw,0,fo,64); bar();
  TC(9,cf) TC(10,cg)
}
DI float gelu(float x){ float u=x*(1.f+0.044715f*x*x)*-2.3022082f; return x*__builtin_amdgcn_rcpf(1.f+__builtin_amdgcn_exp2f(u)); }
DI void roleB2(const Args& A,char* ws,int grp,int j,const int w,int& dead){
  const int l=lane(),g=l>>4,n=l&15;
  rsrc_t rw=mk(ws,WS_SZ); const u32 l16=l*16;
  waitf(rw,O_FL+F_KV+(grp*16+j)*128,1,1,dead);
  u32x4 wo[32][2],wi[16][2];
  const int un=j+16*w,isz2=un>=22,u=un-22*isz2;
  { rsrc_t ro=mk(A.wo,4*MB),ri=mk(A.win,512*1376*2); const u32 base=w*32768,va=g*1024+(n>>2)*16384+(n&3)*16,vb=(g+(n>>2)*8)*2752+(n&(u==21?1:3))*16,
      mk_=(u==21&&n>=8)?0:~0u; u32 ra=base+l*4; PIN(ra);
    stream<24,8>(base,[&](int c,u32 d){ u32 so=c<16?j*64+w*MB:un<44?isz2*1376+u*64:0; asm volatile("":"+s"(so),"+s"(d));
        UNR for(int i=0;i<4;i++){ if(c<16) dmaw(ro,va,so+(c*64+i*4)*1024,d+i*1024); else dmaw(ri,vb,so+((c&7)*64+(i>>1)*32+(i&1)*4)*2752,d+i*1024); } },
      [&](int c,u32 o){ u32 t[16]; u32x4 f0,f1; UNR for(int i=0;i<16;i++){ t[i]=LDS(u32,ra+o+i*256); if(c>=16) t[i]&=mk_; }
        UNR for(int q=0;q<2;q++){ mkfr(t+8*q,f0,f1); if(c<15){ f0=toA(f0); f1=toA(f1); } else { PIN(f0); PIN(f1); }
          if(c<16){ wo[2*c+q][0]=f0; wo[2*c+q][1]=f1; } else { wi[2*(c&7)+q][0]=f0; wi[2*(c&7)+q][1]=f1; } } }); }
  { const u32x4 sv={SENT,SENT,SENT,SENT}; const int c=(grp*16+j)*4+w;
    UNR for(int i=0;i<8;i++) st4c(rw,l16+(i&3)*1024,O_HF+(c*8+(i&4))*1024,sv);
    UNR for(int i=0;i<11;i++){ st4c(rw,l16+(i&3)*1024,O_Z1+(c*11+(i&12))*1024,sv); st4c(rw,l16+(i&3)*1024,O_Z2+(c*11+(i&12))*1024,sv); st4c(rw,l16+(i&3)*1024,O_UF+(c*11+(i&12))*1024,sv); }
    UNR for(int i=0;i<2;i++) st4c(rw,l16,O_ST+(c*2+i)*1024,sv);
    vm0(); bar(); if(w==0) st1c(rw,0,O_FL+F_FI+c*32,1); }
  waitf(rw,O_FL+F_FI,64,1,dead,128);
  TS(1)
  enum{L_X=131072,L_P=147456-2048,L_S=153600};
  const u32x4 one={0x3F803F80u,0x3F803F80u,0x3F803F80u,0x3F803F80u}; f32x4 cs0={},cs1={};
  if(!isz2){ UNR for(int k=0;k<16;k++){ cs0=mfma(wi[k][0],one,cs0); cs1=mfma(wi[k][1],one,cs1); } }
  else     { UNR for(int k=0;k<16;k++){ cs0=mfma(one,wi[k][0],cs0); cs1=mfma(one,wi[k][1],cs1); } }
  u32 lx=L_X+l16,lo=w*32768+l16; PIN(lx); PIN(lo); PIN(cs0); PIN(cs1);
  int cf=0,cg=0; rsrc_t rx=mk(A.x,2*MB); const u32 fv=(l&7)*128+(l&8)/2;
  PT0
  for(int it=0;;it++){
    const int blk=grp+4*it,pb=blk-4; const u32 fo=O_FL+F_O+(grp*32+w*8)*128,need=(blk>>1)+1,ob=O_OF+(blk*128+w*32)*1024;
    bool rdy=false;
    if(it){ const int s=pb>>1,kt=pb&1; int sp=0;
      if(w) __builtin_amdgcn_s_sleep(DH+5); else __builtin_amdgcn_s_sleep(DH);
      PT(0) u32x4 x[16]; u32 fl;
      for(;;){ asm volatile("":::"memory"); UNR for(int i=0;i<4;i++) x[i]=ld4c(rw,l16+i*1024,O_HF+(pb*16+w*4)*1024);
        fl=ld1c(rw,fv,fo); u32 mx=0; UNR for(int i=0;i<4;i++) mx=mx4(mx,x[i]);
        if(dead||!__any(mx==SENT)) break; if(++sp>SPINCAP){dead=1;break;} }
      PT(1) UNR for(int i=0;i<4;i++) LDS(u32x4,lx+(w*4+i)*1024)=x[i];
      rdy=!__any(fl<need); bar();
      UNR for(int k=0;k<16;k++) x[k]=LDS(u32x4,lx+k*1024); SB();
      if(it==32) TS(3)
      f32x4 z0={},z1={};
#define OD if((k&3)==0){ SB(); if(rdy) dmaN<1>(rw,l16,ob+k*1024,w*32768+k*1024); SB(); }
      if(w==3){ UNR for(int k=0;k<16;k++){ z0=mfma(x[k],x[k],z0); z1=mfma(one,x[k],z1); OD }
        float mu=z1.x*(1.f/512),q=(n&2)?((n&1)?z0.w:z0.z):((n&1)?z0.y:z0.x);
        if(g==n>>2){ LDS(float,L_S+n*4)=mu; LDS(float,L_S+64+n*4)=rsqrtf(fmaxf(q*(1.f/512)-mu*mu,0.f)+1e-5f); } }
      else if(!isz2){ UNR for(int k=0;k<16;k++){ z0=mfma(wi[k][0],x[k],z0); z1=mfma(wi[k][1],x[k],z1); OD } }
      else     { UNR for(int k=0;k<16;k++){ z0=mfma(x[k],wi[k][0],z0); z1=mfma(x[k],wi[k][1],z1); OD } }
      bar();
      if(un<44){ f32x4 mu,rs;
        if(isz2){ mu=LDS(f32x4,L_S+g*16); rs=LDS(f32x4,L_S+64+g*16); } else { float a=LDS(float,L_S+n*4),b=LDS(float,L_S+64+n*4); mu=f32x4{a,a,a,a}; rs=f32x4{b,b,b,b}; }
        UNR for(int i=0;i<4;i++){ z0[i]=(z0[i]-mu[i]*cs0[i])*rs[i]; z1[i]=(z1[i]-mu[i]*cs1[i])*rs[i]; }
        UNR for(int i=0;i<4;i++){ z0[i]=gelu(z0[i]); z1[i]=gelu(z1[i]); }
        if(!isz2) st4c(rw,l16,O_Z1+(pb*22+u)*1024,pk8(z0,z1));
        else { u32x4 v=pkc(z0,z1); st4c(rw,l16,O_Z2+(s*44+2*u+kt)*1024,v);
          f32x4 a,b; UNR for(int i=0;i<4;i++){ float p=(i&1)?bhi(v[i>>1]):blo(v[i>>1]),q=(i&1)?bhi(v[2+(i>>1)]):blo(v[2+(i>>1)]); a[i]=rsum16(p+q); b[i]=rsum16(p*p+q*q); }
          if(n<2) st4c(rw,g*32+n*16,O_ST+(s*22+u)*256+kt*128,__builtin_bit_cast(u32x4,n?f32x4{a.z,a.w,b.z,b.w}:f32x4{a.x,a.y,b.x,b.y})); } }
      if(it==32){ TC(9,cf) TC(10,cg) PTE break; }
      PT(2)
    }
    if(w==0) dmaw(rx,(8*(n>>2)+(n&3))*1024+g*16,((blk>>1)*32+4*(blk&1))*1024+j*64,154624);
    if(!rdy){ waitf(rw,fo,64,need,dead,0,fv); CNT(cf,1) if(it==31) TS(2) dmaN<4>(rw,l16,ob,w*32768); }
    PT(3) f32x4 c0={},c1={}; u32x4 o[2][4];
    UNR for(int b=0;b<=8;b++){
      if(b<8) asm volatile("s_waitcnt vmcnt(%0)"::"n"(b<4?12:28-4*b):"memory");
      UNR for(int i=0;i<4;i++){ if(b<8) o[b&1][i]=LDS(u32x4,lo+(b*4+i)*1024); SB();
        if(b>0){ c0=mfma(wo[(b-1)*4+i][0],o[(b-1)&1][i],c0); c1=mfma(wo[(b-1)*4+i][1],o[(b-1)&1][i],c1); } SB(); }
      if(b<4) dmaN<1>(rw,l16,ob+16384+b*4096,w*32768+16384+b*4096);
      SB(); }
    PT(4)
    if(w){ LDS(f32x4,L_P+w*2048+l16)=c0; LDS(f32x4,L_P+w*2048+1024+l16)=c1; }
    bar();
    if(w==0){ UNR for(int q=1;q<4;q++){ c0+=LDS(f32x4,L_P+q*2048+l16); c1+=LDS(f32x4,L_P+q*2048+1024+l16); }
      u32x4 a=pk8(c0,c1),hh,xr=LDS(u32x4,154624+l16); UNR for(int i=0;i<4;i++) hh[i]=pk(blo(a[i])+blo(xr[i]),bhi(a[i])+bhi(xr[i]));
      st4c(rw,l16,O_HF+(blk*16+j)*1024,hh); }
    PT(5)
  }
}
DI void loadF(const Args& A,u32x4 (&wf)[22][2],int nq,const int w){
  const int l=lane(),g=l>>4,n=l&15; rsrc_t ro=mk(A.wout,688*1024);
  const u32 base=w*32768,va=g*1024+(n>>2)*8192+(n&3)*16; u32 ra=base+l*4; PIN(ra); bar();
  stream<11,8>(base,[&](int c,u32 d){ u32 so=nq*256+w*64; asm volatile("":"+s"(so),"+s"(d));
      UNR for(int i=0;i<4;i++) dmaw(ro,(c==10&&i>=2)?va&~16384u:va,so+(c*64+(i>>1)*32+(i&1)*4)*1024,d+i*1024); },
    [&](int c,u32 o){ u32 t[16]; u32x4 f0,f1; UNR for(int i=0;i<16;i++) t[i]=LDS(u32,ra+o+i*256);
      UNR for(int q=0;q<2;q++){ mkfr(t+8*q,f0,f1); if(c<9){ f0=toA(f0); f1=toA(f1); } else { PIN(f0); PIN(f1); } wf[2*c+q][0]=f0; wf[2*c+q][1]=f1; } });
  bar();
}
template<int OWN> DI void roleF(const Args& A,char* ws,u32x4 (&wf)[22][2],int tb,int nq,const int w,int& dead){
  const int l=lane(),g=l>>4,n=l&15;
  rsrc_t rw=mk(ws,WS_SZ); const u32 l16=l*16,ub=O_UF+(tb*44+w*11)*1024; int sp=0;
  if(!OWN){ waitf(rw,O_FL+F_FI,64,1,dead,128); TS(5)
    while(!dead&&__any(ld1c(rw,min(n,10)*1024,ub)==SENT)){ if(++sp>SPINCAP){dead=1;break;} asm volatile("s_sleep 2":::"memory"); }
    TS(6) }
  u32x4 hf[2],uf[11];
  for(;;){ asm volatile("":::"memory"); u32 mx=0;
    if(!OWN){ UNR for(int i=0;i<11;i++) uf[i]=ld4c(rw,l16+(i&3)*1024,ub+(i&12)*1024); }
    UNR for(int i=0;i<2;i++) hf[i]=ld4c(rw,l16,O_HF+((tb*2+i)*16+4*nq+w)*1024);
    if(!OWN){ UNR for(int i=0;i<11;i++){ mx=mx4(mx,uf[i]); LDS(u32x4,(w*11+i)*1024+l16)=uf[i]; } }
    mx=mx4(mx4(mx,hf[0]),hf[1]);
    if(dead||!__any(mx==SENT)) break; if(++sp>SPINCAP){dead=1;break;} }
  bar();
  f32x4 c[2][2]={}; u32x4 x[6]; UNR for(int i=0;i<5;i++) x[i]=LDS(u32x4,((i&1)*22+(i>>1))*1024+l16);
  UNR for(int i=0;i<44;i++){ c[i&1][0]=mfma(wf[i>>1][0],x[i%6],c[i&1][0]); SB();
    if(i+5<44) x[(i+5)%6]=LDS(u32x4,(((i+5)&1)*22+((i+5)>>1))*1024+l16); SB();
    c[i&1][1]=mfma(wf[i>>1][1],x[i%6],c[i&1][1]); SB(); }
  UNR for(int kt=0;kt<2;kt++){ u32x4 a=pk8(c[kt][0],c[kt][1]),v; UNR for(int i=0;i<4;i++) v[i]=pk(blo(a[i])+blo(hf[kt][i]),bhi(a[i])+bhi(hf[kt][i]));
    ((GL u32x4*)A.out)[(tb*32+8*(n>>2)+4*kt+(n&3))*64+nq*16+w*4+g]=v; }
}
DI void roleB3(const Args& A,char* ws,int tb,const int w,int& dead){
  const int l=lane(),g=l>>4,n=l&15;
  rsrc_t rw=mk(ws,WS_SZ),rs_=mk(A.wsp,8*MB); const u32 l16=l*16;
  f32x4 c[12][2]={}; float cr[2]={0,0}; u32x4 z[12],y[12]={}; int cf=0;
  const int tk=tb*32+8*(n>>2)+(n&3); const int nt=w<2?12:10,t0=w<2?12*w:10*w+4;
  { u32 so=w*MB+tb*16384+__builtin_amdgcn_readfirstlane(ld1(rw,0,O_FL+F_SEL+(w>>1)*4));
    if(w<3||tb){ UNR for(int i=0;i<8;i++) st4c(rw,l16+(i&3)*1024,so+(i&4)*1024,u32x4{SENT,SENT,SENT,SENT}); }
    vm0(); bar(); if(w==0) st1c(rw,0,O_FL+F_RI+tb*128,1); }
  UNR for(int i=0;i<DW;i++) __builtin_amdgcn_s_sleep(37);
  u32x4 wf[22][2]; loadF(A,wf,3,w);
  for(int i=0;i<32;i++) dma16(rs_,tk*4096+g*16,(w*16+(i>>2)*2+(i&1))*64+(i&2)*8192,((w*16+(i>>2)*2+(i&1))*2+((i>>1)&1))*1024);
  vm0(); TS(1) waitf(rw,O_FL+F_O+tb*128,1,7,dead); waitf(rw,O_FL+F_FI,64,1,dead,128); bar();
  float bsp[2]; UNR for(int kt=0;kt<2;kt++){ bsp[kt]=blo(((const GL unsigned short*)A.bsp)[tk+4*kt]); PIN(bsp[kt]); }
  const u32 v0=n*256+g*32,v1=n<6?v0+4096:0x7FFFF000u,sb=O_ST+(w>>1)*128+(w&1)*16,lw=l16+w*4; u32x4 ta={SENT},tc={};
  for(int s=0;s<64;s++){
    int sp=0; const bool ly=s==max(tb,61);
#define LDZ { UNR for(int i=0;i<12;i++) if(i<nt) z[i]=ld4c(rw,l16+(i&3)*1024,O_Z2+(s*44+t0+(i&12))*1024); \
      if(ly){ UNR for(int i=0;i<12;i++) if(i<nt) y[i]=ld4c(rw,l16,O_Z1+((tb*2+(i&1))*22+(t0>>1)+(i>>1))*1024); } }
    while(!dead&&__any(mx4(mx4(0,ta),tc)==SENT)){ if(++sp>SPINCAP){dead=1;break;} if(s<61) __builtin_amdgcn_s_sleep(DZ); else if(sp>1) __builtin_amdgcn_s_sleep(DY); ta=ld4c(rw,v0,sb+s*5632); tc=ld4c(rw,v1,sb+s*5632); if(SPZ&&s==63) LDZ }
    f32x4 st=__builtin_bit_cast(f32x4,ta)+__builtin_bit_cast(f32x4,tc); u32 d0=LDS(u32,s*2048+lw),d1=LDS(u32,s*2048+1024+lw); if(s==63) TS(2)
    SB(); if(!(SPZ&&s==63&&sp)) LDZ ta=ld4c(rw,v0,sb+s*5632+5632); tc=ld4c(rw,v1,sb+s*5632+5632); SB();
    UNR for(int i=0;i<4;i++) st[i]=rsum16(st[i])*(1.f/688);
    float ra=rsqrtf(fmaxf(st.z-st.x*st.x,0.f)+1e-5f),rb=rsqrtf(fmaxf(st.w-st.y*st.y,0.f)+1e-5f);
    d0=pk(blo(d0)*ra,bhi(d0)*rb); d1=pk(blo(d1)*ra,bhi(d1)*rb); LDS(u32,s*2048+lw)=d0; LDS(u32,s*2048+1024+lw)=d1;
    cr[0]+=blo(d0)*st.x+bhi(d0)*st.y; cr[1]+=blo(d1)*st.x+bhi(d1)*st.y;
    if(s==63){ UNR for(int kt=0;kt<2;kt++){ float v=gsum(cr[kt]); if(g==0) LDS(float,131072+kt*256+lw)=v; } }
    bar(); const u32x4 b0=LDS(u32x4,s*2048+l16),b1=LDS(u32x4,s*2048+1024+l16); SB();
    for(;;){ u32 mx=0; UNR for(int i=0;i<12;i++) if(i<nt) mx=mx4(mx,z[i]);
      if(ly){ UNR for(int i=0;i<12;i++) if(i<nt) mx=mx4(mx,y[i]); }
      if(dead||!__any(mx==SENT)) break; if(++sp>SPINCAP){dead=1;break;} CNT(cf,1) asm volatile("":::"memory"); LDZ }
    UNR for(int i=0;i<12;i++) if(i<nt){ const int o=(i&1)*2; u32x4 a={z[i&14][o],z[i&14][o+1],z[i|1][o],z[i|1][o+1]}; c[i][0]=mfma(a,b0,c[i][0]); c[i][1]=mfma(a,b1,c[i][1]); }
  }
  TS(3) TC(9,cf)
  UNR for(int kt=0;kt<2;kt++){ f32x4 cc=LDS(f32x4,131072+kt*256+n*16); float bs=bsp[kt]-(cc.x+cc.y+cc.z+cc.w);
    UNR for(int i=0;i<6;i++) if(2*i<nt){ u32x4 v; UNR for(int q=0;q<4;q++) v[q]=pk(blo(y[2*i+kt][q])*(c[2*i][kt][q]+bs),bhi(y[2*i+kt][q])*(c[2*i+1][kt][q]+bs));
      st4c(rw,l16,O_UF+((tb*2+kt)*22+(t0>>1)+i)*1024,v); LDS(u32x4,(kt*22+(t0>>1)+i)*1024+l16)=v; } }
  TS(5) roleF<1>(A,ws,wf,tb,3,w,dead);
}
DI Args AR(){ typedef const __attribute__((address_space(4))) u64* AP; AP p=(AP)__builtin_amdgcn_kernarg_segment_ptr(); asm volatile("":"+s"(p));
  return Args{(void*)p[0],(void*)p[1],(void*)p[2],(void*)p[3],(void*)p[4],(void*)p[5],(void*)p[6],(void*)p[7],(void*)p[8],(void*)p[9],(void*)p[10],(void*)p[11]}; }
#define PFC 1
__global__ void __launch_bounds__(256,1) main_kernel(Args A){
  __shared__ __attribute__((aligned(1024))) char smem[LDS_SZ+1024];
  asm volatile(""::"v"(smem):"memory");
  char* ws=wsbase(A.ws); const int b=blockIdx.x,x=b&7,y=b>>3; int dead=0; const int w=__builtin_amdgcn_readfirstlane(threadIdx.x>>6);
  TS(0)
  { u64 pc; u32 n;
    asm volatile("s_getpc_b64 %0\n.Lmk_beg:\n s_mov_b32 %1, .Lmk_end-.Lmk_beg":"=s"(pc),"=s"(n));
    asm volatile("s_mov_b32 m0, %2\n s_nop 4\n buffer_load_dword %0, %1, 0 offen lds"::"v"((u32)(y*256+threadIdx.x)*64u),"s"(mk((void*)pc,n)),"s"((u32)(LDS_SZ+w*256)):"memory"); }
  if(y>=16&&x<4) roleB3(AR(),ws,x*16+y-16,w,dead);
  else {
    if(x<4) roleR(AR(),ws,8*x+(y>>1),y&1,w,dead); else if(y<16) roleB1(AR(),ws,8*(x-4)+(y>>1),y&1,w,dead); else roleB2(AR(),ws,x-4,y-16,w,dead);
    TS(4) u32x4 wf[22][2]; const Args B=AR(); const int nq=(x>>2)+(y>>4); loadF(B,wf,nq,w); roleF<0>(B,ws,wf,(x&3)*16+(y&15),nq,w,dead);
  }
  TS(7)
  asm volatile(".Lmk_end:");
}
extern "C" void kernel_launch(void* const* d_in,const int* in_sizes,int n_in,void* d_out,int out_size,void* d_ws,size_t ws_size,hipStream_t stream){
  Args A{d_in[0],d_in[1],d_in[2],d_in[3],d_in[4],d_in[5],d_in[8],d_in[9],d_in[10],d_in[11],d_out,d_ws};
  pre_kernel<<<258,256,0,stream>>>(A);
  main_kernel<<<256,256,0,stream>>>(A);
}
```

```cpp
#include <hip/hip_runtime.h>
#include <hip/hip_bf16.h>
typedef unsigned u32;
typedef unsigned long long u64;
typedef __attribute__((ext_vector_type(2))) u32 u32x2;
typedef __attribute__((ext_vector_type(4))) u32 u32x4;
typedef __attribute__((ext_vector_type(8))) u32 u32x8;
typedef __attribute__((ext_vector_type(2))) float f32x2;
typedef __attribute__((ext_vector_type(4))) float f32x4;
typedef __attribute__((ext_vector_type(2))) __bf16 bfx2;
typedef __attribute__((ext_vector_type(8))) __bf16 bfx8;
typedef __amdgpu_buffer_rsrc_t rsrc_t;
#define DI __device__ __forceinline__
#define UNR _Pragma("unroll")
#define SB() __builtin_amdgcn_sched_barrier(0)
#define SENT 0xFFFFFFFFu
#define SPINCAP 400000
#define MB (1u<<20)
#define O_M   (3*MB)
#define O_FL  (4*MB)
#define F_SEL 0
#define F_KV  1024
#define F_KM  (F_KV+8192)
#define F_O   (F_KM+8192)
#define F_RI  (F_O+16384)
#define F_FI  (F_RI+8192)
#define FL_SZ 65536
#define O_XF  (5*MB)
#define O_HF  (7*MB)
#define O_Z1  (9*MB)
#define O_Z2  (12*MB)
#define O_UF  (15*MB)
#define O_ST  (18*MB)
#define O_KM  (20*MB)
#define O_KV  (24*MB)
#define O_VM  (56*MB)
#define O_OF  (72*MB)
#define WS_SZ (88*MB)
#define LDS_SZ (152*1024)

DI rsrc_t mk(const void* p,u32 n){ return __builtin_amdgcn_make_buffer_rsrc((void*)p,0,n,0x00020000); }
DI u32x4 ld4(rsrc_t r,u32 v,u32 s){ return __builtin_amdgcn_raw_buffer_load_b128(r,v,s,0); }
DI u32x2 ld2(rsrc_t r,u32 v,u32 s){ return __builtin_amdgcn_raw_buffer_load_b64(r,v,s,0); }
DI u32   ld1(rsrc_t r,u32 v,u32 s){ return __builtin_amdgcn_raw_buffer_load_b32(r,v,s,0); }
DI u32x4 ld4c(rsrc_t r,u32 v,u32 s){ return __builtin_amdgcn_raw_buffer_load_b128(r,v,s,16); }
DI u32x2 ld2c(rsrc_t r,u32 v,u32 s){ return __builtin_amdgcn_raw_buffer_load_b64(r,v,s,16); }
DI u32   ld1c(rsrc_t r,u32 v,u32 s){ return __builtin_amdgcn_raw_buffer_load_b32(r,v,s,16); }
DI void st4c(rsrc_t r,u32 v,u32 s,u32x4 d){ __builtin_amdgcn_raw_buffer_store_b128(d,r,v,s,16); }
DI void st2c(rsrc_t r,u32 v,u32 s,u32x2 d){ __builtin_amdgcn_raw_buffer_store_b64(d,r,v,s,16); }
DI void st1c(rsrc_t r,u32 v,u32 s,u32 d){ __builtin_amdgcn_raw_buffer_store_b32(d,r,v,s,16); }
DI void dma16(rsrc_t r,u32 v,u32 s,u32 lds){ asm volatile("s_mov_b32 m0, %3\n s_nop 0\n buffer_load_dwordx4 %0, %1, %2 offen sc1 lds"::"v"(v),"s"(r),"s"(s),"s"(lds):"memory"); }
#define SCX "sc1"
template<int N4> DI void dmaN(rsrc_t r,u32 v,u32 s,u32 lds){ asm volatile("s_mov_b32 m0, %3\n .rept %4\n s_nop 0\n .irp o,0,1024,2048,3072\n buffer_load_dwordx4 %1, %2, %0 offen offset:\\o " SCX " lds\n .endr\n s_add_u32 m0,m0,4096\n s_add_u32 %0,%0,4096\n .endr":"+s"(s):"v"(v),"s"(r),"s"(lds),"n"(N4):"memory","scc"); }
DI void vm0(){ asm volatile("s_waitcnt vmcnt(0)":::"memory"); }
DI void bar(){ asm volatile("s_waitcnt lgkmcnt(0)\n s_barrier":::"memory"); }
DI u32 pk(float a,float b){ f32x2 v={a,b}; return __builtin_bit_cast(u32,__builtin_convertvector(v,bfx2)); }
DI float blo(u32 v){ return __uint_as_float(v<<16); }
DI float bhi(u32 v){ return __uint_as_float(v&0xFFFF0000u); }
DI u32 plo(u32 a,u32 b){ return __builtin_amdgcn_perm(b,a,0x05040100u); }
DI u32 phi(u32 a,u32 b){ return __builtin_amdgcn_perm(b,a,0x07060302u); }
DI f32x4 mfma(u32x4 a,u32x4 b,f32x4 c){ return __builtin_amdgcn_mfma_f32_16x16x32_bf16(__builtin_bit_cast(bfx8,a),__builtin_bit_cast(bfx8,b),c,0,0,0); }
DI float gmax(float x){ u32 u=__float_as_uint(x); auto a=__builtin_amdgcn_permlane16_swap(u,u,false,false); x=fmaxf(__uint_as_float(a[0]),__uint_as_float(a[1]));
  u=__float_as_uint(x); auto b=__builtin_amdgcn_permlane32_swap(u,u,false,false); return fmaxf(__uint_as_float(b[0]),__uint_as_float(b[1])); }
DI float gsum(float x){ u32 u=__float_as_uint(x); auto a=__builtin_amdgcn_permlane16_swap(u,u,false,false); x=__uint_as_float(a[0])+__uint_as_float(a[1]);
  u=__float_as_uint(x); auto b=__builtin_amdgcn_permlane32_swap(u,u,false,false); return __uint_as_float(b[0])+__uint_as_float(b[1]); }
template<int C> DI float dppf(float x){ return __int_as_float(__builtin_amdgcn_update_dpp(0,__float_as_int(x),C,0xF,0xF,true)); }
DI float rsum16(float x){ x+=dppf<0x128>(x); x+=dppf<0x124>(x); x+=dppf<0x122>(x); x+=dppf<0x121>(x); return x; }
template<class T> DI T toA(T v){ T o; asm("":"=a"(o):"0"(v)); return o; }
#define LDS(T,off) (*(__attribute__((address_space(3))) T*)(size_t)(off))
DI int lane(){ int l; asm volatile("v_mbcnt_lo_u32_b32 %0, -1, 0\n v_mbcnt_hi_u32_b32 %0, -1, %0":"=v"(l)); __builtin_assume(l>=0&&l<64); return l; }
DI void waitf(rsrc_t r,u32 off,u32 n,u32 need,int& dead,u32 str=4,u32 vo=0){
  if(n>1&&str) vo=(lane()&(n-1))*str; int sp=0;
  while(!dead){ u32 v=ld1c(r,vo,off); if(!__any(v<need)) break; if(++sp>SPINCAP){dead=1;break;} __builtin_amdgcn_s_sleep(2); }
  asm volatile("":::"memory");
}
DI void mkfr(const u32* t,u32x4& f0,u32x4& f1){
  f0=u32x4{plo(t[0],t[1]),plo(t[2],t[3]),plo(t[4],t[5]),plo(t[6],t[7])};
  f1=u32x4{phi(t[0],t[1]),phi(t[2],t[3]),phi(t[4],t[5]),phi(t[6],t[7])};
}
DI void dmaw(rsrc_t r,u32 v,u32 s,u32 lds){ asm volatile("s_mov_b32 m0, %3\n s_nop 0\n buffer_load_dwordx4 %0, %1, %2 offen lds"::"v"(v),"s"(r),"s"(s),"s"(lds):"memory"); }
template<int NC,int NR,class I,class U> DI void stream(u32 base,I issue,U use){
  UNR for(int c=0;c<NR;c++) issue(c,base+c*4096);
  UNR for(int c=0;c<NC;c++){ asm volatile("s_waitcnt vmcnt(%0)"::"n"(4*((NC-c<NR?NC-c:NR)-1)):"memory");
    use(c,(c%NR)*4096); SB(); asm volatile("s_waitcnt lgkmcnt(0)":::"memory");
    if(c+NR<NC) issue(c+NR,base+(c%NR)*4096); SB(); }
}
#define TC(i,v)
#define CNT(c,v)
#define PT(i)
#define PT0
#define PTE
#define TS(i) {}
#undef PT
#undef PT0
#undef PTE
#define PT(i)
#define PT0
#define PTE
DI u32 selof(u32 v,int pg){ u32 m=~0u; UNR for(int i=0;i<8;i++) { u32 r=__builtin_amdgcn_readlane(v,pg*8+i); asm("s_min_u32 %0, %0, %1":"+s"(m):"s"(r):"scc"); } return __builtin_amdgcn_readfirstlane((m&1)*8192); }
struct Args{ const void *x,*mem0,*wq,*wk,*wv,*wo,*win,*wsp,*bsp,*wout; void *out,*ws; };
DI char* wsbase(void* ws){ return (char*)(((size_t)ws+(2*MB-1))&~(size_t)(2*MB-1)); }

#define NCAL 4
#define PSC 0
__global__ void __launch_bounds__(256) pre_kernel(Args A){
  char* ws=wsbase(A.ws); rsrc_t rw=mk(ws,WS_SZ);
  int b=blockIdx.x,t=threadIdx.x,l=t&63,w=__builtin_amdgcn_readfirstlane(t>>6);
  __shared__ u32 tm[8];
  u32x4 xv[2]; TS(22)
  const bool cal=b<32; const int br=b-32;
  if(!cal){ UNR for(int i=0;i<2;i++) xv[i]=((const u32x4*)A.x)[(br*8+w*2+i)*64+l]; }
  else if(!(b&4)){
    const u32 ci=(b>>4)*4+(b&3),pg=(b>>3)&1,cb=(pg*128+ci)*16384+w*2048;
    if(l<NCAL) st1c(rw,((l+(b>>4))&1)*8192+l*256,cb,SENT);
    vm0(); u32 mn=~0u;
    _Pragma("nounroll") for(int i=0;i<NCAL;i++){ const u32 hf=(i+(b>>4))&1,vo=cb+hf*8192+i*256; u64 c0,c1; u32 v;
      asm volatile("s_barrier\n s_memtime %0\n s_waitcnt lgkmcnt(0)\n buffer_load_dword %2, %3, %4, 0 offen " SCX "\n s_waitcnt vmcnt(0)\n s_memtime %1\n s_waitcnt lgkmcnt(0)"
        :"=&s"(c0),"=&s"(c1),"=&v"(v):"v"(vo),"s"(rw):"memory");
      u32 d=(u32)(c1-c0);
      mn=min(mn,d*2+hf); }
    tm[w]=mn; __syncthreads();
    if(t==0) *(u32*)(ws+O_FL+F_SEL+(pg*8+ci)*4)=min(min(tm[0],tm[1]),min(tm[2],tm[3]));
  }
  if(b==192)
  UNR for(int i=0;i<4;i++){ u32x4 v={SENT,SENT,SENT,SENT}; int c=i*256+t;
    { int cc=c&511; const u32x2* p=(const u32x2*)A.mem0+((cc>>2)&7)*128+(cc>>6)*16+(cc&3)*4+((cc>>5)&1); u32x2 a=p[0],e=p[2]; v=u32x4{a.x,a.y,e.x,e.y}; }
    __builtin_amdgcn_raw_buffer_store_b128(v,rw,(b*1024+c)*16,0,PSC); }
  if(b>=4&&b<256&&t<16) ((u32x4*)(ws+O_FL))[b*16+t]=u32x4{0,0,0,0};
  if(!cal) UNR for(int i=0;i<2;i++){
    float f[8],s=0; UNR for(int j=0;j<4;j++){ f[2*j]=blo(xv[i][j]); f[2*j+1]=bhi(xv[i][j]); s+=f[2*j]+f[2*j+1]; }
    float mu=gsum(rsum16(s))*(1.f/512),q=0; UNR for(int j=0;j<8;j++){ f[j]-=mu; q+=f[j]*f[j]; }
    float rs=rsqrtf(gsum(rsum16(q))*(1.f/512)+1e-5f);
    int tk=br*8+w*2+i,tau=tk&31,kt=(tau>>2)&1,c=4*(tau>>3)+(tau&3);
    ((u32x4*)(ws+O_XF))[(((tk>>5)*2+kt)*16+(l>>2))*64+(l&3)*16+c]=u32x4{pk(f[0]*rs,f[1]*rs),pk(f[2]*rs,f[3]*rs),pk(f[4]*rs,f[5]*rs),pk(f[6]*rs,f[7]*rs)};
  }
  TS(23)
}
#define RG 63
#define PIN(x) asm volatile("":"+v"(x))
#define DL1 10
#define DL2 7
#define DL3 0
#define DL4 0
#define OPT 4
#define STR_(x) #x
#define STR(x) STR_(x)
#define CEXP 0.12751743f
typedef __attribute__((ext_vector_type(16))) u32 u32x16;
typedef __attribute__((ext_vector_type(32))) u32 u32x32;
#define MT(i)
#define MTACC
#define CLOB "v128","v129","v130","v131","v132","v133","v134","v135","v136","v137","v138","v139","v140","v141","v142","v143","v144","v145","v146","v147","v148","v149","v150","v151","v152","v153","v154","v155","v156","v157","v158","v159","v160","v161","v162","v163","v164","v165","v166","v167","v168","v169","v170","v171","v172","v173","v174","v175","v176","v177","v178","v179","v180","v181","v182","v183","v184","v185","v186","v187","v188","v189","v190","v191","v192","v193","v194","v195","v196","v197","v198","v199","v200","v201","v202","v203","v204","v205","v206","v207","v208","v209","v210","v211","v212","v213","v214","v215","v216","v217","v218","v219","v220","v221","v222","v223","v224","v225","v226","v227","v228","v229","v230","v231","v232","v233","v234","v235","v236","v237","v238","v239","v240","v241","v242","v243","v244","v245","v246","v247","s40","s41","s42","s43","s44","s45","s46","s47","s48","s49","s50","s51","s52","s53","s54","s55","s56","s57","s58","s59","s60","s61","s62","s63","s80","s81","s82","s83","s84","s85","s86","s87"
DI void roleR(const Args& A,char* ws,int h,int th,const int w,int& dead){
  const int l=lane(),g=l>>4,n=l&15;
  rsrc_t rw=mk(ws,WS_SZ);
  u32x32 wa[8],wv[2],wo[2];
  { rsrc_t rq=mk(A.wq,4*MB),rk=mk(A.wk,4*MB),rv=mk(A.wv,4*MB),ro=mk(A.wo,4*MB);
    const u32 rl=(32*(g>>1)+8*(g&1))*8192+h*256;
#define ROWJ(j) ((16*(((j)>>1)&1)+4*(((j)>>3)&1)+2*(((j)>>2)&1)+((j)&1))*8192)
    u32x2 v[32]; u32x4 u[16];
    UNR for(int j=0;j<32;j++) v[j]=ld2(rv,rl+n*8,(2*w+(j>>4))*64*8192+th*128+ROWJ(j));
    UNR for(int j=0;j<16;j++) u[j]=ld4(ro,g*8192+n*16,(h*128+th*64+(j&7)+32*(j>>3))*1024+w*256);
    const u32 base=w*32768,va=ROWJ(g)+(32*(n>>3)+8*((n>>2)&1))*8192+h*256+(n&3)*16; u32 ra=base+l*4; PIN(ra); u32x32 f;
    stream<16,8>(base,[&](int c,u32 d){ u32 so=((c+2*w)&7)*64*8192+w*64; asm volatile("":"+s"(so),"+s"(d));
        UNR for(int i=0;i<4;i++) dmaw(c<8?rq:rk,va,so+ROWJ(4*i),d+i*1024); },
      [&](int c,u32 o){ u32 p[16]; UNR for(int i=0;i<16;i++) p[i]=LDS(u32,ra+o+i*256);
        UNR for(int j=0;j<16;j++){ int q=(j&7)*2; f[(c&1)*16+j]=(j&8)?phi(p[q],p[q+1]):plo(p[q],p[q+1]); }
#define PA(i,r) if(c==2*i+1) asm volatile("":"={a[" r "]}"(wa[i]):"0"(f));
        PA(0,"0:31") PA(1,"32:63") PA(2,"64:95") PA(3,"96:127") PA(4,"128:159") PA(5,"160:191") PA(6,"192:223") PA(7,"224:255") });
    bar();
    UNR for(int q=0;q<8;q++) UNR for(int j=0;j<8;j++){ const u32x2* p=v+(q>>2)*16+2*j; int hi=(q>>1)&1;
      wv[q>>2][(q&3)*8+j]=(q&1)?phi(p[0][hi],p[1][hi]):plo(p[0][hi],p[1][hi]); }
    asm volatile("":"+{v[0:31]}"(wv[0]),"+{v[32:63]}"(wv[1])); SB();
    UNR for(int q=0;q<8;q++) UNR for(int j=0;j<8;j++) wo[q>>2][(q&3)*8+j]=(q&1)?phi(u[2*j][q>>1],u[2*j+1][q>>1]):plo(u[2*j][q>>1],u[2*j+1][q>>1]);
    asm volatile("":"+{v[64:95]}"(wo[0]),"+{v[96:127]}"(wo[1]));
  }
  const u32 sv=ld1(rw,(lane()&15)*4,O_FL+F_SEL),sel0=selof(sv,0),sel1=selof(sv,1);
  const int pi=2*h+th,p=(n>>1)&1; const u32 l16=l*16;
  { u32x4 sel; UNR for(int j=0;j<4;j++) sel[j]=(n&3)==j?((n&4)?0x3F800000u:0x3F80u):0u; LDS(u32x4,17408+l16)=sel; LDS(u32x4,18432)=u32x4{0x3F803F80u,0x3F803F80u,0x3F80u,0}; }
  const int rho=(2*w+(n>>3))*8+g;
  u32x8 vc={p?0xEEEEu:0x4444u, l16, rho*16384+((pi+rho+(rho&3))&63)*128+(n&7)*16u, 8192+2*g*288+w*64+n*4u,
            8192+(2*(n>>2)+(n&1))*288+g*16u, 32768+l16, w*2048+((g>>1)*4+(n>>2))*128+(n&1)*64+(2*(g&1)+p)*16u,
            w==0?g*64+n*8u:w==3?10496+(4*th+g)*288+n*16u:13312+(w-1)*2048+l16};
  const u32 kvs=O_KV+h*16384+w*3072;
  TS(1) waitf(rw,O_FL+F_RI,64,1,dead,128); waitf(rw,O_FL+F_KV+2*h*128,1,1,dead); TS(2) TC(20,__builtin_amdgcn_s_memtime())
  UNR for(int i=0;i<3;i++) dma16(rw,l16,kvs+i*1024+(3*w+i>=8?th*4096:0),32768+(3*w+i)*1024);
  u32x16 sc={(u32)w, sel0, MB+sel0, 2*MB+sel1, O_M+sel1, pi*16384u, O_KV+h*16384+MB/2+(w-1+(w==3?th:0))*4096, pi*16384u+(w==3?5120:(w-1)*3072),
             O_FL+F_KV+(2*h+1)*128u, O_FL+F_KM+pi*128u, w==3?O_KM+h*2048+th*1024:O_VM+pi*4096+(w-1)*2048u, w==3?65536u:262144u,
             49152+(w-1)*4096u, (u32)dead, 0, 0};
  u32x8 pc={};
  asm volatile(
".macro SQ c,a,w\n"
" v_smfmac_f32_16x16x64_bf16 v[\\c:\\c+3],v[\\a:\\a+3],a[\\w:\\w+7],v248\n"
".endm\n"
".macro SV c,a,w\n"
" v_smfmac_f32_16x16x64_bf16 v[\\c:\\c+3],v[\\a:\\a+3],v[\\w:\\w+7],v248\n"
".endm\n"
".macro MZ d,a,b\n"
" v_mfma_f32_16x16x32_bf16 v[\\d:\\d+3],v[\\a:\\a+3],v[\\b:\\b+3],0\n"
".endm\n"
".macro MA d,a,b\n"
" v_mfma_f32_16x16x32_bf16 v[\\d:\\d+3],v[\\a:\\a+3],v[\\b:\\b+3],v[\\d:\\d+3]\n"
".endm\n"
".macro RD d,a,o\n"
" ds_read_b128 v[\\d:\\d+3],v\\a offset:\\o\n"
".endm\n"
".macro AD2 c\n"
" v_add_f32 v[\\c],v[\\c],v[\\c+2]\n"
" v_add_f32 v[\\c+1],v[\\c+1],v[\\c+3]\n"
".endm\n"
".macro CV d,a,b\n"
" v_cvt_pk_bf16_f32 v[\\d],v[\\a],v[\\b]\n"
".endm\n"
".macro ZR a,n\n"
" .set zi,\\a\n"
" .rept \\n\n"
" v_mov_b64 v[zi:zi+1],0\n"
" .set zi,zi+2\n"
" .endr\n"
".endm\n"
".macro SPIN lab,inc\n"
" s_cmp_eq_u32 s80,0\n"
" s_cselect_b32 s81,\\inc,0\n"
" s_add_u32 s79,s79,s81\n"
" s_add_u32 s80,s80,1\n"
" s_cmp_gt_u32 s80," STR(SPINCAP) "\n"
" s_cselect_b32 s77,1,s77\n"
" s_cmp_lg_u32 s77,0\n"
" s_cbranch_scc1 \\lab\n"
".endm\n"
".macro EXPORT\n"
" RD 144,255,0\n"
" RD 148,255,1024\n"
" s_waitcnt lgkmcnt(0)\n"
" buffer_store_dwordx4 v[144:147],v249,%3,s74 offen sc1\n"
" s_cmp_eq_u32 s64,3\n"
" s_cbranch_scc1 1f\n"
" buffer_store_dwordx4 v[148:151],v249,%3,s74 offen offset:1024 sc1\n"
"1:\n"
".endm\n"
" s_mov_b32 s56,0xCCCCCCCC\n"
" s_mov_b32 s57,0xCCCCCCCC\n"
" s_lshl_b32 s58,s64,11\n"
" s_add_u32 s59,s58,2048\n"
" s_and_b32 s59,s59,8191\n"
" s_xor_b32 s60,s58,4096\n"
" s_xor_b32 s61,s59,4096\n"
" s_lshr_b32 s63,s64,1\n"
" s_lshl_b32 s63,s63,10\n"
" s_and_b32 s82,s64,1\n"
" s_lshl_b32 s82,s82,2\n"
" s_add_u32 s63,s63,s82\n"
" s_mov_b32 s87,0x3e0293ee\n"
" ZR 168,16\n"
" ZR 216,16\n"
" v_mov_b32 v203,0\n"
" s_waitcnt vmcnt(0) lgkmcnt(0)\n"
"RL_loop:\n"
" v_add_u32 v160,s58,v249\n"
" v_add_u32 v161,s59,v249\n"
" v_add_u32 v162,s60,v249\n"
" v_add_u32 v163,s61,v249\n"
" s_add_u32 s84,s65,65536\n"
" s_mov_b32 m0,s76\n"
" s_add_u32 s86,s78,2\n"
" s_min_u32 s86,s86,62\n"
" s_cmp_eq_u32 s64,0\n"
" s_cselect_b32 s86,0,s86\n"
" v_mov_b32 v202,s78\n"
" v_mov_b64 v[204:205],-1\n"
" v_mov_b64 v[206:207],-1\n"
" s_add_u32 s82,s67,s69\n"
" s_add_u32 s82,s82,s58\n"
" s_mov_b32 s80,0\n"
MT(40)
" buffer_load_dwordx4 v[128:131],v254,%3,s68 offen sc1\n"
" buffer_load_dwordx4 v[132:135],v254,%3,s68 offen offset:1024 sc1\n"
" s_waitcnt vmcnt(0)\n"
"RL_retry:\n"
MT(42)
" ds_write_b128 v160,v[128:131]\n"
" SQ 168,128,0\n"
" ds_write_b128 v160,v[132:135] offset:1024\n"
" v_max3_u32 v200,v128,v129,v130\n"
" SQ 172,128,8\n"
" v_max3_u32 v201,v131,v132,v133\n"
" v_max3_u32 v200,v200,v134,v135\n"
" SQ 168,132,16\n"
" v_max_u32 v200,v200,v201\n"
" v_cmp_eq_u32 vcc,-1,v200\n"
" s_cbranch_vccnz RL_failm\n"
"RL_okm:\n"
" SV 184,128,0\n"
" v_add_u32 v201,s63,v249\n"
" SV 184,132,32\n"
" v_readfirstlane_b32 s85,v203\n"
" SV 188,128,8\n"
" SV 188,132,40\n"
" SV 192,128,16\n"
" AD2 184\n"
" SV 192,132,48\n"
" CV 184,184,185\n"
" SV 196,128,24\n"
" AD2 188\n"
" SV 196,132,56\n"
" CV 188,188,189\n"
" s_cmp_lg_u32 s64,3\n"
" s_waitcnt lgkmcnt(0)\n"
" s_barrier\n"
" RD 136,161,0\n"
" RD 140,161,1024\n"
" SQ 172,132,24\n"
" RD 144,162,0\n"
" SQ 176,128,128\n"
" RD 148,162,1024\n"
" SQ 180,128,136\n"
" RD 152,163,0\n"
" SQ 176,132,144\n"
" RD 156,163,1024\n"
" SQ 180,132,152\n"
" s_cbranch_scc1 3f\n"
" buffer_store_dword v202,off,%3,s73 sc1\n"
"3:\n"
" AD2 192\n"
" s_waitcnt lgkmcnt(5)\n"
" SQ 168,136,32\n"
" AD2 196\n"
" SQ 172,136,40\n"
" CV 192,192,193\n"
" CV 196,196,197\n"
" s_waitcnt lgkmcnt(0)\n"
" SQ 168,140,48\n"
" .if (" STR(OPT) "&20)==4\n"
" buffer_store_dwordx4 v[204:207],v249,%3,s82 offen sc1\n"
" .endif\n"
" ds_write_b32 v201,v184 offset:13312\n"
" SQ 172,140,56\n"
" .if " STR(OPT) "&8\n"
" s_cmp_lt_u32 s85,s86\n"
" s_cbranch_scc1 RL_failf\n"
"RL_okf:\n"
" s_cmp_eq_u32 s64,0\n"
" .endif\n"
" ds_write_b32 v201,v188 offset:15360\n"
" SQ 168,144,64\n"
" ds_write_b32 v201,v192 offset:13320\n"
" SQ 172,144,72\n"
" .if " STR(OPT) "&8\n"
" s_cbranch_scc1 5f\n"
" buffer_load_dwordx4 v249,%3,s70 offen offset:0 sc1 lds\n"
"5:\n"
" .endif\n"
" RD 128,253,0\n"
" SQ 168,148,80\n"
" .if (" STR(OPT) "&20)==4\n"
" buffer_store_dwordx4 v[204:207],v249,%3,s82 offen offset:1024 sc1\n"
" .endif\n"
" RD 132,253,4096\n"
" SQ 172,148,88\n"
" ds_write_b32 v201,v196 offset:15368\n"
" SQ 168,152,96\n"
" .if " STR(OPT) "&8\n"
" s_cbranch_scc1 5f\n"
" buffer_load_dwordx4 v249,%3,s70 offen offset:1024 sc1 lds\n"
"5:\n"
" .endif\n"
" RD 160,253,1024\n"
" SQ 168,156,112\n"
" RD 164,253,5120\n"
" SQ 172,152,104\n"
" .if " STR(OPT) "&8\n"
" s_cbranch_scc1 5f\n"
" buffer_load_dwordx4 v249,%3,s70 offen offset:2048 sc1 lds\n"
"5:\n"
" .endif\n"
" RD 184,253,2048\n"
" SQ 172,156,120\n"
" RD 188,253,6144\n"
" SQ 176,136,160\n"
" AD2 168\n"
" SQ 180,136,168\n"
" .if " STR(OPT) "&8\n"
" s_cbranch_scc1 5f\n"
" buffer_load_dwordx4 v249,%3,s70 offen offset:3072 sc1 lds\n"
"5:\n"
" .endif\n"
" RD 192,253,3072\n"
" SQ 176,140,176\n"
" AD2 172\n"
" SQ 180,140,184\n"
" CV 168,168,172\n"
" CV 169,169,173\n"
" SQ 176,144,192\n"
" ds_write2_b32 v251,v168,v169 offset1:72\n"
" SQ 180,144,200\n"
" RD 196,253,7168\n"
" SQ 176,148,208\n"
" .if " STR(OPT) "&16\n"
" buffer_store_dwordx4 v[204:207],v249,%3,s82 offen sc1\n"
" .endif\n"
" SQ 180,148,216\n"
" .if " STR(OPT) "&16\n"
" buffer_store_dwordx4 v[204:207],v249,%3,s82 offen offset:1024 sc1\n"
" .endif\n"
" s_waitcnt lgkmcnt(1)\n"
" s_barrier\n"
" RD 200,252,0\n"
" SQ 176,152,224\n"
" RD 204,252,64\n"
" SQ 176,156,240\n"
" RD 208,252,128\n"
" SQ 180,152,232\n"
" RD 212,252,192\n"
" s_mov_b64 vcc,s[56:57]\n"
" SQ 180,156,248\n"
MT(44)
" s_cmp_eq_u32 s78,63\n"
" AD2 176\n"
" s_waitcnt lgkmcnt(3)\n"
" MZ 168,128,200\n"
" MZ 172,132,200\n"
" s_waitcnt lgkmcnt(2)\n"
" AD2 180\n"
" MA 168,160,204\n"
" CV 176,176,180\n"
" CV 177,177,181\n"
" ds_write_b32 v251,v176 offset:2304\n"
" ds_write_b32 v251,v177 offset:2592\n"
" MA 172,164,204\n"
" s_waitcnt lgkmcnt(2)\n"
" MA 168,184,208\n"
" MA 172,188,208\n"
" s_waitcnt lgkmcnt(0)\n"
" s_barrier\n"
" s_cbranch_scc1 RL_last\n"
" RD 136,252,2304\n"
" RD 140,252,2368\n"
" MA 168,192,212\n"
" RD 144,252,2432\n"
" RD 148,252,2496\n"
" MA 172,196,212\n"
" RD 152,253,8192\n"
" RD 156,253,9216\n"
" RD 128,253,10240\n"
" RD 132,253,11264\n"
" RD 196,216,18432\n"
" s_waitcnt lgkmcnt(8)\n"
" MZ 176,136,200\n"
" s_nop 0\n"
" v_cndmask_b32 v168,v168,v172,vcc\n"
" v_cndmask_b32 v169,v169,v173,vcc\n"
" s_waitcnt lgkmcnt(7)\n"
" MA 176,140,204\n"
" v_cndmask_b32 v170,v170,v174,vcc\n"
" v_cndmask_b32 v171,v171,v175,vcc\n"
" s_waitcnt lgkmcnt(5)\n"
" MA 176,144,208\n"
" v_max3_f32 v180,v168,v169,v170\n"
" v_max_f32 v180,v180,v171\n"
" MA 176,148,212\n"
" RD 200,249,13312\n"
" RD 204,249,14336\n"
" v_max_f32_dpp v180,v180,v180 quad_perm:[2,3,0,1] row_mask:0xf bank_mask:0xf\n"
" RD 208,249,15360\n"
" RD 212,249,16384\n"
" s_nop 2\n"
MT(46)
" v_max3_f32 v181,v180,v176,v177\n"
" v_max3_f32 v180,v180,v176,v177\n"
" v_cndmask_b32 v182,v176,v177,vcc\n"
" s_nop 0\n"
" v_permlane16_swap_b32 v180,v181\n"
" v_max_f32 v183,v180,v181\n"
" v_max_f32 v180,v180,v181\n"
" s_nop 1\n"
" v_permlane32_swap_b32 v180,v183\n"
" v_max_f32 v180,v180,v183\n"
" v_mul_f32 v180,s87,v180\n"
" v_fma_f32 v168,v168,s87,-v180\n"
" v_fma_f32 v169,v169,s87,-v180\n"
" v_exp_f32 v168,v168\n"
" v_fma_f32 v170,v170,s87,-v180\n"
" v_exp_f32 v169,v169\n"
" v_fma_f32 v171,v171,s87,-v180\n"
" v_exp_f32 v170,v170\n"
" v_exp_f32 v171,v171\n"
" CV 160,168,169\n"
" CV 161,170,171\n"
" v_cndmask_b32 v172,v160,v216,vcc\n"
" v_cndmask_b32 v173,v161,v216,vcc\n"
" v_cndmask_b32_dpp v174,v160,v216,vcc quad_perm:[2,3,0,1] row_mask:0xf bank_mask:0xf\n"
" v_cndmask_b32_dpp v175,v161,v216,vcc quad_perm:[2,3,0,1] row_mask:0xf bank_mask:0xf\n"
" v_fma_f32 v182,v182,s87,-v180\n"
" s_waitcnt lgkmcnt(0)\n"
" MZ 184,152,172\n"
" v_exp_f32 v182,v182\n"
" v_mov_b32_dpp v164,v172 quad_perm:[2,3,0,1] row_mask:0xf bank_mask:0xf\n"
" MZ 188,156,172\n"
" v_mov_b32_dpp v165,v173 quad_perm:[2,3,0,1] row_mask:0xf bank_mask:0xf\n"
" v_mov_b32_dpp v166,v174 quad_perm:[2,3,0,1] row_mask:0xf bank_mask:0xf\n"
" v_mov_b32_dpp v167,v175 quad_perm:[2,3,0,1] row_mask:0xf bank_mask:0xf\n"
" v_mov_b32_dpp v181,v182 quad_perm:[2,3,0,1] row_mask:0xf bank_mask:0xf\n"
" CV 162,182,181\n"
" MA 184,128,164\n"
" v_cndmask_b32 v168,v162,v216,vcc\n"
" v_cndmask_b32 v169,v162,v216,vcc\n"
" MZ 192,196,160\n"
" v_mov_b32_dpp v170,v168 quad_perm:[2,3,0,1] row_mask:0xf bank_mask:0xf\n"
" v_mov_b32_dpp v171,v168 quad_perm:[2,3,0,1] row_mask:0xf bank_mask:0xf\n"
" MA 188,132,164\n"
" s_nop 0\n"
" MA 184,200,168\n"
" MA 184,204,168\n"
" v_add_f32_dpp v192,v192,v192 quad_perm:[2,3,0,1] row_mask:0xf bank_mask:0xf\n"
" MA 188,208,168\n"
" v_rcp_f32 v192,v192\n"
" MA 188,212,168\n"
" s_nop 3\n"
" v_mul_f32 v184,v184,v192\n"
" v_mul_f32 v185,v185,v192\n"
" v_mul_f32 v186,v186,v192\n"
" v_mul_f32 v187,v187,v192\n"
" v_mul_f32 v188,v188,v192\n"
" v_mul_f32 v189,v189,v192\n"
" v_mul_f32 v190,v190,v192\n"
" v_mul_f32 v191,v191,v192\n"
" CV 172,184,188\n"
" CV 173,185,189\n"
" CV 174,186,190\n"
" CV 175,187,191\n"
MT(48)
" s_nop 1\n"
" SV 216,172,64\n"
" SV 220,172,72\n"
" SV 224,172,80\n"
" SV 228,172,88\n"
" AD2 216\n"
" CV 128,216,217\n"
" SV 232,172,96\n"
" AD2 220\n"
" CV 129,220,221\n"
" SV 236,172,104\n"
" AD2 224\n"
" CV 130,224,225\n"
" SV 240,172,112\n"
" AD2 228\n"
" CV 131,228,229\n"
" SV 244,172,120\n"
" buffer_store_dwordx4 v[128:131],v250,%3,s65 offen sc1\n"
" AD2 232\n"
" AD2 236\n"
" AD2 240\n"
" CV 132,232,233\n"
" AD2 244\n"
" CV 133,236,237\n"
" CV 134,240,241\n"
" CV 135,244,245\n"
" buffer_store_dwordx4 v[132:135],v250,%3,s84 offen sc1\n"
MT(50)
" RD 160,249,17408\n"
" ZR 168,16\n"
" ZR 216,16\n"
" s_add_u32 s68,s68,16384\n"
" s_cmp_lg_u32 s64,0\n"
" s_cbranch_scc1 RL_other\n"
" s_add_u32 s82,s65,s69\n"
" s_add_u32 s83,s82,4096\n"
" s_lshr_b32 s84,s69,7\n"
" s_add_u32 s84,s84,s68\n"
" s_mov_b32 s80,0\n"
" s_waitcnt lgkmcnt(0)\n"
" s_sleep " STR(DL1) "\n"
MT(52)
"RL_red:\n"
" buffer_load_dwordx4 v[128:131],v249,%3,s82 offen sc1\n"
" buffer_load_dwordx4 v[132:135],v249,%3,s83 offen sc1\n"
" buffer_load_dwordx4 v[136:139],v249,%3,s82 offen offset:1024 sc1\n"
" buffer_load_dwordx4 v[140:143],v249,%3,s83 offen offset:1024 sc1\n"
" buffer_load_dwordx4 v[144:147],v249,%3,s82 offen offset:2048 sc1\n"
" buffer_load_dwordx4 v[148:151],v249,%3,s83 offen offset:2048 sc1\n"
" buffer_load_dwordx4 v[152:155],v249,%3,s82 offen offset:3072 sc1\n"
" buffer_load_dwordx4 v[156:159],v249,%3,s83 offen offset:3072 sc1\n"
" s_waitcnt vmcnt(7)\n"
" MZ 164,160,128\n"
" s_waitcnt vmcnt(6)\n"
" MA 164,160,132\n"
" s_waitcnt vmcnt(5)\n"
" MA 164,160,136\n"
" s_waitcnt vmcnt(4)\n"
" MA 164,160,140\n"
" s_waitcnt vmcnt(3)\n"
" MA 164,160,144\n"
" s_waitcnt vmcnt(2)\n"
" MA 164,160,148\n"
" s_waitcnt vmcnt(1)\n"
" MA 164,160,152\n"
" s_waitcnt vmcnt(0)\n"
" MA 164,160,156\n"
" s_nop 7\n"
" v_cmp_u_f32 vcc,v164,v164\n"
" v_add_f32_dpp v164,v164,v164 row_ror:8 row_mask:0xf bank_mask:0xf\n"
" v_add_f32_dpp v165,v165,v165 row_ror:8 row_mask:0xf bank_mask:0xf\n"
" v_add_f32_dpp v166,v166,v166 row_ror:8 row_mask:0xf bank_mask:0xf\n"
" v_add_f32_dpp v167,v167,v167 row_ror:8 row_mask:0xf bank_mask:0xf\n"
" s_cbranch_vccnz RL_failr\n"
"RL_okr:\n"
" s_mov_b64 exec,0x00FF00FF\n"
" CV 164,164,165\n"
" CV 165,166,167\n"
" buffer_store_dwordx2 v[164:165],v255,%3,s84 offen sc1\n"
" s_mov_b64 exec,-1\n"
"RL_join:\n"
MT(54)
" s_barrier\n"
" s_add_u32 s78,s78,1\n"
" s_mov_b32 s81,s67\n"
" s_mov_b32 s67,s65\n"
" s_mov_b32 s65,s66\n"
" s_mov_b32 s66,s81\n"
" v_xor_b32 v253,16384,v253\n"
MTACC
" s_sleep " STR(DL2) "\n"
" s_branch RL_loop\n"
"RL_other:\n"
" v_mov_b64 v[164:165],-1\n"
" v_mov_b64 v[166:167],-1\n"
" s_add_u32 s82,s67,s71\n"
" .if (" STR(OPT) "&8)==0\n"
" s_cmp_lt_u32 s85,s86\n"
" s_cbranch_scc1 RL_failf\n"
"RL_okf:\n"
" .endif\n"
" s_xor_b32 s72,s72,128\n"
" s_sleep " STR(DL3) "\n"
" .if (" STR(OPT) "&9)==0\n"
" buffer_load_dwordx4 v249,%3,s70 offen sc1 lds\n"
" buffer_load_dwordx4 v249,%3,s70 offen offset:1024 sc1 lds\n"
" buffer_load_dwordx4 v249,%3,s70 offen offset:2048 sc1 lds\n"
" buffer_load_dwordx4 v249,%3,s70 offen offset:3072 sc1 lds\n"
" .endif\n"
" buffer_load_dword v203,off,%3,s72 sc1\n"
" s_sleep " STR(DL4) "\n"
" .if (" STR(OPT) "&2)==0\n"
" EXPORT\n"
" .endif\n"
" .if (" STR(OPT) "&4)==0\n"
" buffer_store_dwordx4 v[164:167],v249,%3,s82 offen sc1\n"
" buffer_store_dwordx4 v[164:167],v249,%3,s82 offen offset:1024 sc1\n"
" buffer_store_dwordx4 v[164:167],v249,%3,s82 offen offset:2048 sc1\n"
" .endif\n"
" s_add_u32 s70,s70,0x80000\n"
" s_add_u32 s74,s74,s75\n"
" .if " STR(RG) "==7\n"
" s_bitset0_b32 s70,22\n"
" s_lshl_b32 s81,s75,3\n"
" s_andn2_b32 s74,s74,s81\n"
" .endif\n"
" s_xor_b32 s76,s76,16384\n"
" s_branch RL_join\n"
"RL_failm:\n"
" SPIN RL_okm,1\n"
" buffer_load_dwordx4 v[128:131],v254,%3,s68 offen sc1\n"
" buffer_load_dwordx4 v[132:135],v254,%3,s68 offen offset:1024 sc1\n"
" ZR 168,4\n"
" s_waitcnt vmcnt(0)\n"
" s_branch RL_retry\n"
"RL_failr:\n"
" .set zi,129\n"
" .rept 15\n"
" v_max3_u32 v128,v128,v[zi],v[zi+1]\n"
" .set zi,zi+2\n"
" .endr\n"
" v_max_u32 v128,v128,v159\n"
" v_cmp_eq_u32 vcc,-1,v128\n"
" s_cbranch_vccz RL_okr\n"
" SPIN RL_okr,1024\n"
" s_branch RL_red\n"
"RL_failf:\n"
" SPIN RL_okf,0x100000\n"
" buffer_load_dword v200,off,%3,s72 sc1\n"
" s_waitcnt vmcnt(0)\n"
" v_readfirstlane_b32 s85,v200\n"
" s_nop 3\n"
" s_cmp_lt_u32 s85,s86\n"
" s_cbranch_scc1 RL_failf\n"
" s_branch RL_okf\n"
"RL_last:\n"
" s_cmp_eq_u32 s64,0\n"
" s_cbranch_scc1 2f\n"
" s_waitcnt lgkmcnt(0)\n"
" EXPORT\n"
"2:\n"
" v_mov_b32 v202,64\n"
" s_waitcnt vmcnt(0) lgkmcnt(0)\n"
" s_barrier\n"
" buffer_store_dword v202,off,%3,s73 sc1\n"
" s_nop 7\n"
  :"+{s[64:79]}"(sc),"+{v[248:255]}"(vc),"+{s[88:95]}"(pc)
  :"s"(rw),"{a[0:31]}"(wa[0]),"{a[32:63]}"(wa[1]),"{a[64:95]}"(wa[2]),"{a[96:127]}"(wa[3]),"{a[128:159]}"(wa[4]),"{a[160:191]}"(wa[5]),"{a[192:223]}"(wa[6]),"{a[224:255]}"(wa[7]),
   "{v[0:31]}"(wv[0]),"{v[32:63]}"(wv[1]),"{v[64:95]}"(wo[0]),"{v[96:127]}"(wo[1])
  :"memory","scc","vcc",CLOB);
  dead=sc[13];
  TC(21,__builtin_amdgcn_s_memtime()) TS(3) TC(9,sc[15]&1023) TC(10,(sc[15]>>10)&1023)
}
#define SPZ 0
#define DY 3
#define DW 4
#define GL __attribute__((address_space(1)))
#define XB 0
#define DH 8
#define DZ 24
DI u32 mx4(u32 m,u32x4 v){ return max(max(max(m,v.x),v.y),max(v.z,v.w)); }
DI u32x4 pk8(f32x4 a,f32x4 b){ return u32x4{pk(a.x,b.x),pk(a.y,b.y),pk(a.z,b.z),pk(a.w,b.w)}; }
DI u32x4 pkc(f32x4 a,f32x4 b){ return u32x4{pk(a.x,a.y),pk(a.z,a.w),pk(b.x,b.y),pk(b.z,b.w)}; }
DI void roleB1(const Args& A,char* ws,int h,int par,const int w,int& dead){
  const int l=lane(),g=l>>4,n=l&15;
  rsrc_t rw=mk(ws,WS_SZ); const u32 l16=l*16;
  enum{LA=3,L_B=65536,L_M=139264};
  UNR for(int i=0;i<8;i++) dma16(rw,l16,O_XF+par*32768+(w*8+i)*1024,(w*8+i)*1024);
  u32x4 wq[16][2],wk[16][2],wv[16][2];
  { rsrc_t rk=mk(A.wk,4*MB),rv=mk(A.wv,4*MB),rq=mk(A.wq,4*MB); const u32 base=32768+w*28672;
    const u32 va=g*8192+(n>>2)*65536+(n&3)*16,vb=(l>>5)*8192+((l>>3)&3)*65536+(l&7)*16; u32 ra=base+l*4,rb=base+l*8+(w&1)*4; PIN(ra); PIN(rb);
    stream<32,7>(base,[&](int c,u32 d){ u32 so=h*256; asm volatile("":"+s"(so),"+s"(d));
        UNR for(int i=0;i<4;i++){ if(c>=8&&c<24) dmaw(rv,vb,so+(w>>1)*128+((c-8)*32+2*i)*8192,d+i*1024);
          else dmaw(c<8?rk:rq,va,so+w*64+((c&7)*64+(i>>1)*32+(i&1)*4)*8192,d+i*1024); } },
      [&](int c,u32 o){ u32 t[16]; u32x4 f0,f1;
        if(c>=8&&c<24){ UNR for(int i=0;i<8;i++) t[i]=LDS(u32,rb+o+i*512); mkfr(t,f0,f1);
          if(c<17){ f0=toA(f0); f1=toA(f1); } else { PIN(f0); PIN(f1); } wv[c-8][0]=f0; wv[c-8][1]=f1; }
        else { UNR for(int i=0;i<16;i++) t[i]=LDS(u32,ra+o+i*256);
          UNR for(int q=0;q<2;q++){ mkfr(t+8*q,f0,f1); if(c<8){ wk[2*c+q][0]=toA(f0); wk[2*c+q][1]=toA(f1); } else { PIN(f0); PIN(f1); wq[2*(c&7)+q][0]=f0; wq[2*(c&7)+q][1]=f1; } } } }); }
  int bi=0,cf=0,cg=0; const u32 fo=O_FL+F_O+((par*2+(w&1))*32+h)*128+(w>>1)*4; vm0(); TS(1)
  for(int i=0;i<32+LA;i++){
    const int s=2*i+par,sa=s-2*LA; const u32 lb=L_B+bi*24576+l16; u32x4 pq[2],pk_[2],pv[2];
    bool pub=i>LA; u32 fl=pub?LDS(u32,151552+w*1024+l16):0; bar();
    if(i>=LA){
      if(__any(fl<(u32)sa+1)){ if(pub){ vm0(); st1c(rw,0,fo,sa-1); pub=false; } waitf(rw,O_FL+F_KM+2*h*128,2,sa+1,dead,128); CNT(cf,1) }
      if(i==32) TS(2) if(i==34) TS(3)
    }
#define KMD(j) { if(j==0) dma16(rw,(2*(n>>2)+(n&1))*256+g*16,O_KM+((sa&RG)*32+h)*2048+w*64,L_M+w*1024); \
      else dma16(rw,l16,O_VM+((sa&RG)*32+h)*8192+(2*w+j-1)*1024,L_M+4096+(2*w+j-1)*1024); }
    if(i<32){
      f32x4 cq[2][2]={},ck[2][2]={},cv[2][2]={}; u32 xs=O_XF+(s+2)*32768+w*8192,xd=((i+1)&1)*32768+w*8192;
      u32x4 xx[2][2]; const u32 xb=(i&1)*32768+l16; xx[0][0]=LDS(u32x4,xb); xx[0][1]=LDS(u32x4,xb+16384);
      UNR for(int ks=0;ks<16;ks++){
        UNR for(int kt=0;kt<2;kt++){ u32x4 x=xx[ks&1][kt];
          ck[kt][0]=mfma(wk[ks][0],x,ck[kt][0]); SB(); xx[(ks+1)&1][kt]=LDS(u32x4,xb+(((ks+1)&15)+16*kt)*1024); SB();
          cv[kt][0]=mfma(x,wv[ks][0],cv[kt][0]); ck[kt][1]=mfma(wk[ks][1],x,ck[kt][1]); cv[kt][1]=mfma(x,wv[ks][1],cv[kt][1]); SB(); }
        if(ks<3) KMD(ks) else if(ks<11){ dma16(rw,l16,xs,xd); xs+=1024; xd+=1024; asm volatile("":"+s"(xs),"+s"(xd)); }
        SB(); }
      const u32 gb=O_KV+((s&RG)*32+h)*16384;
      UNR for(int ks=0;ks<16;ks++){ const int j=ks>>3,q=ks&3;
        UNR for(int kt=0;kt<2;kt++){ u32x4 x=xx[ks&1][kt];
          cq[kt][0]=mfma(wq[ks][0],x,cq[kt][0]); SB(); if(ks<15) xx[(ks+1)&1][kt]=LDS(u32x4,xb+(ks+1+16*kt)*1024); SB();
          cq[kt][1]=mfma(wq[ks][1],x,cq[kt][1]); SB();
          if(kt==0){ if(ks&4) pv[j][q]=pk(cv[q>>1][j][(q&1)*2],cv[q>>1][j][(q&1)*2+1]); else pk_[j][q]=pk(ck[j][0][q],ck[j][1][q]); SB(); } }
        if(ks==3){ vm0(); if(pub) st1c(rw,0,fo,sa-1); pub=false; }
        if(q==3){ if(ks&4) st4c(rw,l16,gb+8192+(2*w+j)*1024,pv[j]); else st4c(rw,l16,gb+(j*4+w)*1024,pk_[j]); }
        SB(); }
      UNR for(int j=0;j<2;j++) pq[j]=toA(pk8(cq[j][0],cq[j][1]));

    } else { KMD(0) KMD(1) KMD(2) }
    if(i>=LA){
      const int qt=w&1,eh=w>>1; f32x4 s0={},s1={},sm={}; u32x4 qf[4],kf[4];
      UNR for(int k=0;k<4;k++){ qf[k]=LDS(u32x4,lb+16384+(qt*4+k)*1024); kf[k]=LDS(u32x4,lb+k*1024); } SB();
      UNR for(int k=0;k<4;k++){ s0=mfma(kf[k],qf[k],s0); SB(); kf[k]=LDS(u32x4,lb+(4+k)*1024); SB(); }
      UNR for(int k=0;k<4;k++) s1=mfma(kf[k],qf[k],s1);
      SB(); if(i>=32) vm0();
      if(pub) st1c(rw,0,fo,sa-1);
      dma16(rw,(l16&16)*8,O_FL+F_KM+2*h*128,151552+w*1024); bar();
      UNR for(int k=0;k<4;k++) kf[k]=LDS(u32x4,L_M+k*1024+l16); SB();
      UNR for(int k=0;k<4;k++){ sm=mfma(kf[k],qf[k],sm); SB(); kf[k]=LDS(u32x4,lb+8192+(eh*4+k)*1024); qf[k]=LDS(u32x4,L_M+4096+(eh*4+k)*1024+l16); SB(); }
      const int dq=8*(n>>2)+4*qt+(n&3)-8*g; float pe[10],mx=fmaxf(sm.x,sm.y)*CEXP;
      UNR for(int j=0;j<8;j++){ pe[j]=j<=dq?(j<4?s0[j&3]:s1[j&3])*CEXP:-1.4426950e9f; mx=fmaxf(mx,pe[j]); }
      pe[8]=sm.x*CEXP; pe[9]=sm.y*CEXP; mx=gmax(mx); float su=0;
      UNR for(int j=0;j<10;j++){ pe[j]=__builtin_amdgcn_exp2f(pe[j]-mx); su+=pe[j]; }
      su=__builtin_amdgcn_rcpf(gsum(su));
      u32x4 pf={pk(pe[0],pe[1]),pk(pe[2],pe[3]),pk(pe[4],pe[5]),pk(pe[6],pe[7])}; u32 p1=pk(pe[8],pe[9]); u32x4 pm={p1,p1,p1,p1};
      f32x4 d[4];
      UNR for(int t=0;t<4;t++){ d[t]=mfma(kf[t],pf,f32x4{0,0,0,0});
        d[t]=mfma(u32x4{qf[(t&1)*2][t&2],qf[(t&1)*2][(t&2)+1],qf[(t&1)*2+1][t&2],qf[(t&1)*2+1][(t&2)+1]},pm,d[t])*su; }
      SB(); vm0();
      UNR for(int j=0;j<2;j++) st4c(rw,l16,O_OF+((2*sa+qt)*128+h*4+eh*2+j)*1024,
        u32x4{pk(d[0][2*j],d[1][2*j]),pk(d[2][2*j],d[3][2*j]),pk(d[0][2*j+1],d[1][2*j+1]),pk(d[2][2*j+1],d[3][2*j+1])});
    } else vm0();
    if(i<32){ bar();
      if(w==0) st1c(rw,0,O_FL+F_KV+(2*h+par)*128,s+1);
      UNR for(int j=0;j<2;j++){ LDS(u32x4,lb+(j*4+w)*1024)=pk_[j]; LDS(u32x4,lb+8192+(2*w+j)*1024)=pv[j]; LDS(u32x4,lb+16384+(j*4+w)*1024)=pq[j]; } }
    bi=bi==LA-1?0:bi+1;
  }
  vm0(); st1c(rw,0,fo,64); bar();
  TC(9,cf) TC(10,cg)
}
DI float gelu(float x){ float u=x*(1.f+0.044715f*x*x)*-2.3022082f; return x*__builtin_amdgcn_rcpf(1.f+__builtin_amdgcn_exp2f(u)); }
DI void roleB2(const Args& A,char* ws,int grp,int j,const int w,int& dead){
  const int l=lane(),g=l>>4,n=l&15;
  rsrc_t rw=mk(ws,WS_SZ); const u32 l16=l*16;
  waitf(rw,O_FL+F_KV+(grp*16+j)*128,1,1,dead);
  u32x4 wo[32][2],wi[16][2];
  const int un=j+16*w,isz2=un>=22,u=un-22*isz2;
  { rsrc_t ro=mk(A.wo,4*MB),ri=mk(A.win,512*1376*2); const u32 base=w*32768,va=g*1024+(n>>2)*16384+(n&3)*16,vb=(g+(n>>2)*8)*2752+(n&(u==21?1:3))*16,
      mk_=(u==21&&n>=8)?0:~0u; u32 ra=base+l*4; PIN(ra);
    stream<24,8>(base,[&](int c,u32 d){ u32 so=c<16?j*64+w*MB:un<44?isz2*1376+u*64:0; asm volatile("":"+s"(so),"+s"(d));
        UNR for(int i=0;i<4;i++){ if(c<16) dmaw(ro,va,so+(c*64+i*4)*1024,d+i*1024); else dmaw(ri,vb,so+((c&7)*64+(i>>1)*32+(i&1)*4)*2752,d+i*1024); } },
      [&](int c,u32 o){ u32 t[16]; u32x4 f0,f1; UNR for(int i=0;i<16;i++){ t[i]=LDS(u32,ra+o+i*256); if(c>=16) t[i]&=mk_; }
        UNR for(int q=0;q<2;q++){ mkfr(t+8*q,f0,f1); if(c<15){ f0=toA(f0); f1=toA(f1); } else { PIN(f0); PIN(f1); }
          if(c<16){ wo[2*c+q][0]=f0; wo[2*c+q][1]=f1; } else { wi[2*(c&7)+q][0]=f0; wi[2*(c&7)+q][1]=f1; } } }); }
  { const u32x4 sv={SENT,SENT,SENT,SENT}; const int c=(grp*16+j)*4+w;
    UNR for(int i=0;i<8;i++) st4c(rw,l16+(i&3)*1024,O_HF+(c*8+(i&4))*1024,sv);
    UNR for(int i=0;i<11;i++){ st4c(rw,l16+(i&3)*1024,O_Z1+(c*11+(i&12))*1024,sv); st4c(rw,l16+(i&3)*1024,O_Z2+(c*11+(i&12))*1024,sv); st4c(rw,l16+(i&3)*1024,O_UF+(c*11+(i&12))*1024,sv); }
    UNR for(int i=0;i<2;i++) st4c(rw,l16,O_ST+(c*2+i)*1024,sv);
    vm0(); bar(); if(w==0) st1c(rw,0,O_FL+F_FI+c*32,1); }
  waitf(rw,O_FL+F_FI,64,1,dead,128);
  TS(1)
  enum{L_X=131072,L_P=147456-2048,L_S=153600};
  const u32x4 one={0x3F803F80u,0x3F803F80u,0x3F803F80u,0x3F803F80u}; f32x4 cs0={},cs1={};
  if(!isz2){ UNR for(int k=0;k<16;k++){ cs0=mfma(wi[k][0],one,cs0); cs1=mfma(wi[k][1],one,cs1); } }
  else     { UNR for(int k=0;k<16;k++){ cs0=mfma(one,wi[k][0],cs0); cs1=mfma(one,wi[k][1],cs1); } }
  u32 lx=L_X+l16,lo=w*32768+l16; PIN(lx); PIN(lo); PIN(cs0); PIN(cs1);
  int cf=0,cg=0; rsrc_t rx=mk(A.x,2*MB); const u32 fv=(l&7)*128+(l&8)/2;
  PT0
  for(int it=0;;it++){
    const int blk=grp+4*it,pb=blk-4; const u32 fo=O_FL+F_O+(grp*32+w*8)*128,need=(blk>>1)+1,ob=O_OF+(blk*128+w*32)*1024;
    bool rdy=false;
    if(it){ const int s=pb>>1,kt=pb&1; int sp=0;
      if(w) __builtin_amdgcn_s_sleep(DH+5); else __builtin_amdgcn_s_sleep(DH);
      PT(0) u32x4 x[16]; u32 fl;
      for(;;){ asm volatile("":::"memory"); UNR for(int i=0;i<4;i++) x[i]=ld4c(rw,l16+i*1024,O_HF+(pb*16+w*4)*1024);
        fl=ld1c(rw,fv,fo); u32 mx=0; UNR for(int i=0;i<4;i++) mx=mx4(mx,x[i]);
        if(dead||!__any(mx==SENT)) break; if(++sp>SPINCAP){dead=1;break;} }
      PT(1) UNR for(int i=0;i<4;i++) LDS(u32x4,lx+(w*4+i)*1024)=x[i];
      rdy=!__any(fl<need); bar();
      UNR for(int k=0;k<16;k++) x[k]=LDS(u32x4,lx+k*1024); SB();
      if(it==32) TS(3)
      f32x4 z0={},z1={};
#define OD if((k&3)==0){ SB(); if(rdy) dmaN<1>(rw,l16,ob+k*1024,w*32768+k*1024); SB(); }
      if(w==3){ UNR for(int k=0;k<16;k++){ z0=mfma(x[k],x[k],z0); z1=mfma(one,x[k],z1); OD }
        float mu=z1.x*(1.f/512),q=(n&2)?((n&1)?z0.w:z0.z):((n&1)?z0.y:z0.x);
        if(g==n>>2){ LDS(float,L_S+n*4)=mu; LDS(float,L_S+64+n*4)=rsqrtf(fmaxf(q*(1.f/512)-mu*mu,0.f)+1e-5f); } }
      else if(!isz2){ UNR for(int k=0;k<16;k++){ z0=mfma(wi[k][0],x[k],z0); z1=mfma(wi[k][1],x[k],z1); OD } }
      else     { UNR for(int k=0;k<16;k++){ z0=mfma(x[k],wi[k][0],z0); z1=mfma(x[k],wi[k][1],z1); OD } }
      bar();
      if(un<44){ f32x4 mu,rs;
        if(isz2){ mu=LDS(f32x4,L_S+g*16); rs=LDS(f32x4,L_S+64+g*16); } else { float a=LDS(float,L_S+n*4),b=LDS(float,L_S+64+n*4); mu=f32x4{a,a,a,a}; rs=f32x4{b,b,b,b}; }
        UNR for(int i=0;i<4;i++){ z0[i]=(z0[i]-mu[i]*cs0[i])*rs[i]; z1[i]=(z1[i]-mu[i]*cs1[i])*rs[i]; }
        UNR for(int i=0;i<4;i++){ z0[i]=gelu(z0[i]); z1[i]=gelu(z1[i]); }
        if(!isz2) st4c(rw,l16,O_Z1+(pb*22+u)*1024,pk8(z0,z1));
        else { u32x4 v=pkc(z0,z1); st4c(rw,l16,O_Z2+(s*44+2*u+kt)*1024,v);
          f32x4 a,b; UNR for(int i=0;i<4;i++){ float p=(i&1)?bhi(v[i>>1]):blo(v[i>>1]),q=(i&1)?bhi(v[2+(i>>1)]):blo(v[2+(i>>1)]); a[i]=rsum16(p+q); b[i]=rsum16(p*p+q*q); }
          if(n<2) st4c(rw,g*32+n*16,O_ST+(s*22+u)*256+kt*128,__builtin_bit_cast(u32x4,n?f32x4{a.z,a.w,b.z,b.w}:f32x4{a.x,a.y,b.x,b.y})); } }
      if(it==32){ TC(9,cf) TC(10,cg) PTE break; }
      PT(2)
    }
    if(w==0) dmaw(rx,(8*(n>>2)+(n&3))*1024+g*16,((blk>>1)*32+4*(blk&1))*1024+j*64,154624);
    if(!rdy){ waitf(rw,fo,64,need,dead,0,fv); CNT(cf,1) if(it==31) TS(2) dmaN<4>(rw,l16,ob,w*32768); }
    PT(3) f32x4 c0={},c1={}; u32x4 o[2][4];
    UNR for(int b=0;b<=8;b++){
      if(b<8) asm volatile("s_waitcnt vmcnt(%0)"::"n"(b<4?12:28-4*b):"memory");
      UNR for(int i=0;i<4;i++){ if(b<8) o[b&1][i]=LDS(u32x4,lo+(b*4+i)*1024); SB();
        if(b>0){ c0=mfma(wo[(b-1)*4+i][0],o[(b-1)&1][i],c0); c1=mfma(wo[(b-1)*4+i][1],o[(b-1)&1][i],c1); } SB(); }
      if(b<4) dmaN<1>(rw,l16,ob+16384+b*4096,w*32768+16384+b*4096);
      SB(); }
    PT(4)
    if(w){ LDS(f32x4,L_P+w*2048+l16)=c0; LDS(f32x4,L_P+w*2048+1024+l16)=c1; }
    bar();
    if(w==0){ UNR for(int q=1;q<4;q++){ c0+=LDS(f32x4,L_P+q*2048+l16); c1+=LDS(f32x4,L_P+q*2048+1024+l16); }
      u32x4 a=pk8(c0,c1),hh,xr=LDS(u32x4,154624+l16); UNR for(int i=0;i<4;i++) hh[i]=pk(blo(a[i])+blo(xr[i]),bhi(a[i])+bhi(xr[i]));
      st4c(rw,l16,O_HF+(blk*16+j)*1024,hh); }
    PT(5)
  }
}
DI void loadF(const Args& A,u32x4 (&wf)[22][2],int nq,const int w){
  const int l=lane(),g=l>>4,n=l&15; rsrc_t ro=mk(A.wout,688*1024);
  const u32 base=w*32768,va=g*1024+(n>>2)*8192+(n&3)*16; u32 ra=base+l*4; PIN(ra); bar();
  stream<11,8>(base,[&](int c,u32 d){ u32 so=nq*256+w*64; asm volatile("":"+s"(so),"+s"(d));
      UNR for(int i=0;i<4;i++) dmaw(ro,(c==10&&i>=2)?va&~16384u:va,so+(c*64+(i>>1)*32+(i&1)*4)*1024,d+i*1024); },
    [&](int c,u32 o){ u32 t[16]; u32x4 f0,f1; UNR for(int i=0;i<16;i++) t[i]=LDS(u32,ra+o+i*256);
      UNR for(int q=0;q<2;q++){ mkfr(t+8*q,f0,f1); if(c<9){ f0=toA(f0); f1=toA(f1); } else { PIN(f0); PIN(f1); } wf[2*c+q][0]=f0; wf[2*c+q][1]=f1; } });
  bar();
}
template<int OWN> DI void roleF(const Args& A,char* ws,u32x4 (&wf)[22][2],int tb,int nq,const int w,int& dead){
  const int l=lane(),g=l>>4,n=l&15;
  rsrc_t rw=mk(ws,WS_SZ); const u32 l16=l*16,ub=O_UF+(tb*44+w*11)*1024; int sp=0;
  if(!OWN){ waitf(rw,O_FL+F_FI,64,1,dead,128); TS(5)
    while(!dead&&__any(ld1c(rw,min(n,10)*1024,ub)==SENT)){ if(++sp>SPINCAP){dead=1;break;} asm volatile("s_sleep 2":::"memory"); }
    TS(6) }
  u32x4 hf[2],uf[11];
  for(;;){ asm volatile("":::"memory"); u32 mx=0;
    if(!OWN){ UNR for(int i=0;i<11;i++) uf[i]=ld4c(rw,l16+(i&3)*1024,ub+(i&12)*1024); }
    UNR for(int i=0;i<2;i++) hf[i]=ld4c(rw,l16,O_HF+((tb*2+i)*16+4*nq+w)*1024);
    if(!OWN){ UNR for(int i=0;i<11;i++){ mx=mx4(mx,uf[i]); LDS(u32x4,(w*11+i)*1024+l16)=uf[i]; } }
    mx=mx4(mx4(mx,hf[0]),hf[1]);
    if(dead||!__any(mx==SENT)) break; if(++sp>SPINCAP){dead=1;break;} }
  bar();
  f32x4 c[2][2]={}; u32x4 x[6]; UNR for(int i=0;i<5;i++) x[i]=LDS(u32x4,((i&1)*22+(i>>1))*1024+l16);
  UNR for(int i=0;i<44;i++){ c[i&1][0]=mfma(wf[i>>1][0],x[i%6],c[i&1][0]); SB();
    if(i+5<44) x[(i+5)%6]=LDS(u32x4,(((i+5)&1)*22+((i+5)>>1))*1024+l16); SB();
    c[i&1][1]=mfma(wf[i>>1][1],x[i%6],c[i&1][1]); SB(); }
  UNR for(int kt=0;kt<2;kt++){ u32x4 a=pk8(c[kt][0],c[kt][1]),v; UNR for(int i=0;i<4;i++) v[i]=pk(blo(a[i])+blo(hf[kt][i]),bhi(a[i])+bhi(hf[kt][i]));
    ((GL u32x4*)A.out)[(tb*32+8*(n>>2)+4*kt+(n&3))*64+nq*16+w*4+g]=v; }
}
DI void roleB3(const Args& A,char* ws,int tb,const int w,int& dead){
  const int l=lane(),g=l>>4,n=l&15;
  rsrc_t rw=mk(ws,WS_SZ),rs_=mk(A.wsp,8*MB); const u32 l16=l*16;
  f32x4 c[12][2]={}; float cr[2]={0,0}; u32x4 z[12],y[12]={}; int cf=0;
  const int tk=tb*32+8*(n>>2)+(n&3); const int nt=w<2?12:10,t0=w<2?12*w:10*w+4;
  { u32 so=w*MB+tb*16384+selof(ld1(rw,(lane()&7)*4,O_FL+F_SEL+(w>>1)*32),0);
    if(w<3||tb){ UNR for(int i=0;i<8;i++) st4c(rw,l16+(i&3)*1024,so+(i&4)*1024,u32x4{SENT,SENT,SENT,SENT}); }
    vm0(); bar(); if(w==0) st1c(rw,0,O_FL+F_RI+tb*128,1); }
  UNR for(int i=0;i<DW;i++) __builtin_amdgcn_s_sleep(37);
  u32x4 wf[22][2]; loadF(A,wf,3,w);
  for(int i=0;i<32;i++) dma16(rs_,tk*4096+g*16,(w*16+(i>>2)*2+(i&1))*64+(i&2)*8192,((w*16+(i>>2)*2+(i&1))*2+((i>>1)&1))*1024);
  vm0(); TS(1) waitf(rw,O_FL+F_O+tb*128,1,7,dead); waitf(rw,O_FL+F_FI,64,1,dead,128); bar();
  float bsp[2]; UNR for(int kt=0;kt<2;kt++){ bsp[kt]=blo(((const GL unsigned short*)A.bsp)[tk+4*kt]); PIN(bsp[kt]); }
  const u32 v0=n*256+g*32,v1=n<6?v0+4096:0x7FFFF000u,sb=O_ST+(w>>1)*128+(w&1)*16,lw=l16+w*4; u32x4 ta={SENT},tc={};
  for(int s=0;s<64;s++){
    int sp=0; const bool ly=s==max(tb,61);
#define LDZ { UNR for(int i=0;i<12;i++) if(i<nt) z[i]=ld4c(rw,l16+(i&3)*1024,O_Z2+(s*44+t0+(i&12))*1024); \
      if(ly){ UNR for(int i=0;i<12;i++) if(i<nt) y[i]=ld4c(rw,l16,O_Z1+((tb*2+(i&1))*22+(t0>>1)+(i>>1))*1024); } }
    while(!dead&&__any(mx4(mx4(0,ta),tc)==SENT)){ if(++sp>SPINCAP){dead=1;break;} if(s<61) __builtin_amdgcn_s_sleep(DZ); else if(sp>1) __builtin_amdgcn_s_sleep(DY); ta=ld4c(rw,v0,sb+s*5632); tc=ld4c(rw,v1,sb+s*5632); if(SPZ&&s==63) LDZ }
    f32x4 st=__builtin_bit_cast(f32x4,ta)+__builtin_bit_cast(f32x4,tc); u32 d0=LDS(u32,s*2048+lw),d1=LDS(u32,s*2048+1024+lw); if(s==63) TS(2)
    SB(); if(!(SPZ&&s==63&&sp)) LDZ ta=ld4c(rw,v0,sb+s*5632+5632); tc=ld4c(rw,v1,sb+s*5632+5632); SB();
    UNR for(int i=0;i<4;i++) st[i]=rsum16(st[i])*(1.f/688);
    float ra=rsqrtf(fmaxf(st.z-st.x*st.x,0.f)+1e-5f),rb=rsqrtf(fmaxf(st.w-st.y*st.y,0.f)+1e-5f);
    d0=pk(blo(d0)*ra,bhi(d0)*rb); d1=pk(blo(d1)*ra,bhi(d1)*rb); LDS(u32,s*2048+lw)=d0; LDS(u32,s*2048+1024+lw)=d1;
    cr[0]+=blo(d0)*st.x+bhi(d0)*st.y; cr[1]+=blo(d1)*st.x+bhi(d1)*st.y;
    if(s==63){ UNR for(int kt=0;kt<2;kt++){ float v=gsum(cr[kt]); if(g==0) LDS(float,131072+kt*256+lw)=v; } }
    bar(); const u32x4 b0=LDS(u32x4,s*2048+l16),b1=LDS(u32x4,s*2048+1024+l16); SB();
    for(;;){ u32 mx=0; UNR for(int i=0;i<12;i++) if(i<nt) mx=mx4(mx,z[i]);
      if(ly){ UNR for(int i=0;i<12;i++) if(i<nt) mx=mx4(mx,y[i]); }
      if(dead||!__any(mx==SENT)) break; if(++sp>SPINCAP){dead=1;break;} CNT(cf,1) asm volatile("":::"memory"); LDZ }
    UNR for(int i=0;i<12;i++) if(i<nt){ const int o=(i&1)*2; u32x4 a={z[i&14][o],z[i&14][o+1],z[i|1][o],z[i|1][o+1]}; c[i][0]=mfma(a,b0,c[i][0]); c[i][1]=mfma(a,b1,c[i][1]); }
  }
  TS(3) TC(9,cf)
  UNR for(int kt=0;kt<2;kt++){ f32x4 cc=LDS(f32x4,131072+kt*256+n*16); float bs=bsp[kt]-(cc.x+cc.y+cc.z+cc.w);
    UNR for(int i=0;i<6;i++) if(2*i<nt){ u32x4 v; UNR for(int q=0;q<4;q++) v[q]=pk(blo(y[2*i+kt][q])*(c[2*i][kt][q]+bs),bhi(y[2*i+kt][q])*(c[2*i+1][kt][q]+bs));
      st4c(rw,l16,O_UF+((tb*2+kt)*22+(t0>>1)+i)*1024,v); LDS(u32x4,(kt*22+(t0>>1)+i)*1024+l16)=v; } }
  TS(5) roleF<1>(A,ws,wf,tb,3,w,dead);
}
DI Args AR(){ typedef const __attribute__((address_space(4))) u64* AP; AP p=(AP)__builtin_amdgcn_kernarg_segment_ptr(); asm volatile("":"+s"(p));
  return Args{(void*)p[0],(void*)p[1],(void*)p[2],(void*)p[3],(void*)p[4],(void*)p[5],(void*)p[6],(void*)p[7],(void*)p[8],(void*)p[9],(void*)p[10],(void*)p[11]}; }
__global__ void __launch_bounds__(256,1) main_kernel(Args A){
  __shared__ __attribute__((aligned(1024))) char smem[LDS_SZ];
  asm volatile(""::"v"(smem):"memory");
  char* ws=wsbase(A.ws); const int b=blockIdx.x,x=b&7,y=b>>3; int dead=0; const int w=__builtin_amdgcn_readfirstlane(threadIdx.x>>6);
  TS(0)
  if(y>=16&&x<4) roleB3(AR(),ws,x*16+y-16,w,dead);
  else {
    if(x<4) roleR(AR(),ws,8*x+(y>>1),y&1,w,dead); else if(y<16) roleB1(AR(),ws,8*(x-4)+(y>>1),y&1,w,dead); else roleB2(AR(),ws,x-4,y-16,w,dead);
    TS(4) u32x4 wf[22][2]; const Args B=AR(); const int nq=(x>>2)+(y>>4); loadF(B,wf,nq,w); roleF<0>(B,ws,wf,(x&3)*16+(y&15),nq,w,dead);
  }
  TS(7)
}
extern "C" void kernel_launch(void* const* d_in,const int* in_sizes,int n_in,void* d_out,int out_size,void* d_ws,size_t ws_size,hipStream_t stream){
  Args A{d_in[0],d_in[1],d_in[2],d_in[3],d_in[4],d_in[5],d_in[8],d_in[9],d_in[10],d_in[11],d_out,d_ws};
  pre_kernel<<<288,256,0,stream>>>(A);
  main_kernel<<<256,256,0,stream>>>(A);
}
```
